```python
import torch

def _softmax(x, axis=-1):
    return torch.softmax(x, dim=axis)
def _gelu(x):
    return torch.nn.functional.gelu(x, approximate="tanh")
def _silu(x):
    return torch.nn.functional.silu(x)
def _sigmoid(x):
    return torch.sigmoid(x)
def _layernorm(x, axis=-1, eps=1e-5):
    m = x.mean(dim=axis, keepdim=True)
    v = x.var(dim=axis, keepdim=True, unbiased=False)
    return (x - m) / torch.sqrt(v + eps)
def _scatter_add(tgt, idx, src, axis=0):
    return tgt.clone().index_add_(axis, idx, src)
def _segment_sum(src, seg, n):
    out = torch.zeros((n,) + src.shape[1:], dtype=src.dtype)
    return out.index_add_(0, seg, src)
def _pad(x, pw):
    flat = tuple(v for pair in reversed(pw) for v in pair)
    return torch.nn.functional.pad(x, flat)

kernel_name = 'parallel_sum__ssm_conv_s4d__geglu_ffn__prompt'

def setup_inputs():

    _w = (lambda a: (lambda t: t.to(torch.bfloat16) if t.is_floating_point() else t)(torch.as_tensor(a)))
    def _A_setup():
        Bsz, L, D, N = 1, 2048, 512, 128
        return {
            "u": _w((lambda *s: torch.randn(*s, dtype=torch.bfloat16) * 0.05)(Bsz, L, D)),
            "log_A_real": _w((lambda *s: torch.randn(*s, dtype=torch.bfloat16) * 3.2)(D, N) * 0.5),
            "A_imag": _w((lambda *s: torch.randn(*s, dtype=torch.bfloat16) * 0.1)(D, N) * 3.14),
            "C_real": _w((lambda *s: torch.randn(*s, dtype=torch.bfloat16) * 1.6)(D, N) / N ** 0.5),
            "C_imag": _w((lambda *s: torch.randn(*s, dtype=torch.bfloat16) * 1.6)(D, N) / N ** 0.5),
            "log_dt": _w((lambda *s: torch.randn(*s, dtype=torch.bfloat16) * 0.1)(D) * 0.5 - 4.0),
            "D_skip": _w((lambda *s: torch.randn(*s, dtype=torch.bfloat16) * 0.2)(D)),
        }
    _A_in = _A_setup()

    def _B_setup():
        B, S, D, F = 1, 2048, 512, 1376
        return {
            "x": _w((lambda *s: torch.randn(*s, dtype=torch.bfloat16) * 0.05)(B, S, D)),
            "w_gate": _w((lambda *s: torch.randn(*s, dtype=torch.bfloat16) * 0.05)(D, F)),
            "w_up": _w((lambda *s: torch.randn(*s, dtype=torch.bfloat16) * 0.05)(D, F)),
            "w_down": _w((lambda *s: torch.randn(*s, dtype=torch.bfloat16) * 0.05)(F, D)),
        }
    _B_in = _B_setup()

    return {
        "A_u": _A_in["u"],
        "A_log_A_real": _A_in["log_A_real"],
        "A_A_imag": _A_in["A_imag"],
        "A_C_real": _A_in["C_real"],
        "A_C_imag": _A_in["C_imag"],
        "A_log_dt": _A_in["log_dt"],
        "A_D_skip": _A_in["D_skip"],
        "B_x": _B_in["x"],
        "B_w_gate": _B_in["w_gate"],
        "B_w_up": _B_in["w_up"],
        "B_w_down": _B_in["w_down"],
    }

def reference(A_u, A_log_A_real, A_A_imag, A_C_real, A_C_imag, A_log_dt, A_D_skip, B_x, B_w_gate, B_w_up, B_w_down):
    def _A_ref(u, log_A_real, A_imag, C_real, C_imag, log_dt, D_skip):
        L = u.shape[1]
        dt = torch.exp(log_dt)
        A = -torch.exp(log_A_real) + 1j * A_imag
        C = (C_real + 1j * C_imag) * dt[:, None]
        pos = (lambda x, dt: x.to(dt))(torch.arange(L), log_dt.dtype)
        P = torch.exp((A * dt[:, None])[:, :, None] * pos[None, None, :])
        K = 2.0 * torch.einsum("dn,dnl->dl", C, P).real
        L2 = 2 * L
        u_t = (lambda x, *d: x.permute(*d))(u, 0, 2, 1)
        Kf = (lambda x, *a, **k: torch.fft.rfft(x.float(), *a, **k))(K, L2)
        Uf = (lambda x, *a, **k: torch.fft.rfft(x.float(), *a, **k))(u_t, L2)
        y = (lambda x, *a, **k: torch.fft.irfft(x, *a, **k).to(torch.bfloat16))(Uf * Kf[None, :, :], L2)[:, :, :L]
        y = (lambda x, *d: x.permute(*d))(y, 0, 2, 1)
        return y + u * D_skip[None, None, :]

    def _B_ref(x, w_gate, w_up, w_down):
        return (_gelu(x @ w_gate) * (x @ w_up)) @ w_down

    x = A_u
    xn = _layernorm(x, axis=-1)
    return x + _A_ref(xn, A_log_A_real, A_A_imag, A_C_real, A_C_imag, A_log_dt, A_D_skip) + _B_ref(xn, B_w_gate, B_w_up, B_w_down)
```

```cpp
#include <hip/hip_runtime.h>
#include <hip/hip_bf16.h>

#define LAS __attribute__((address_space(3)))
#define GAS __attribute__((address_space(1)))
typedef unsigned short u16;
typedef unsigned int u32;
typedef short bf16x8 __attribute__((ext_vector_type(8)));
typedef short s16x4 __attribute__((ext_vector_type(4)));
typedef float f32x4 __attribute__((ext_vector_type(4)));
typedef float f32x2 __attribute__((ext_vector_type(2)));
typedef unsigned u32x4 __attribute__((ext_vector_type(4)));
typedef unsigned u32x2 __attribute__((ext_vector_type(2)));
typedef __bf16 bf16x2_t __attribute__((ext_vector_type(2)));

constexpr int SL = 2048, DM = 512, NS = 128, FF = 1376;
constexpr int XS = 2080;
constexpr size_t XT_OFF = 0;
constexpr size_t ST_OFF = 2134016;
constexpr size_t H_OFF  = ST_OFF + 8192;
constexpr size_t YT_OFF = H_OFF + (size_t)32 * 22 * 8192;
constexpr size_t WS_END = YT_OFF + (size_t)512 * 2048 * 2;

struct Params {
    const u16 *x, *logA, *aim, *cre, *cim, *logdt, *dskip, *wg, *wu, *wd;
    u16* out; char* ws;
};

__device__ __forceinline__ float bf2f(u32 b) { return __uint_as_float(b << 16); }
__device__ __forceinline__ float bflo(u32 v) { return __uint_as_float(v << 16); }
__device__ __forceinline__ float bfhi(u32 v) { return __uint_as_float(v & 0xffff0000u); }
__device__ __forceinline__ u32 pk2(float a, float b) { bf16x2_t v = {(__bf16)a, (__bf16)b}; return __builtin_bit_cast(u32, v); }
__device__ __forceinline__ float rbf(float a) { return __uint_as_float(pk2(a, a) << 16); }
template <int CTRL, int RM> __device__ __forceinline__ float dpp_add(float v) {
    return v + __int_as_float(__builtin_amdgcn_update_dpp(0, __float_as_int(v), CTRL, RM, 0xf, true));
}
__device__ __forceinline__ float wave_sum(float v) {
    v = dpp_add<0x111, 0xf>(v); v = dpp_add<0x112, 0xf>(v); v = dpp_add<0x114, 0xf>(v); v = dpp_add<0x118, 0xf>(v);
    v = dpp_add<0x142, 0xa>(v); v = dpp_add<0x143, 0xc>(v);
    return __int_as_float(__builtin_amdgcn_readlane(__float_as_int(v), 63));
}
__device__ __forceinline__ f32x4 mfma16(bf16x8 a, bf16x8 b, f32x4 c) { return __builtin_amdgcn_mfma_f32_16x16x32_bf16(a, b, c, 0, 0, 0); }
__device__ __forceinline__ void glds16(const void* g, LAS char* l) { __builtin_amdgcn_global_load_lds((const GAS u32*)g, (LAS u32*)l, 16, 0, 0); }
typedef short v4i16_t __attribute__((ext_vector_type(4)));
__device__ __forceinline__ s16x4 trrd(LAS char* p) { return __builtin_amdgcn_ds_read_tr16_b64_v4i16((LAS v4i16_t*)p); }
__device__ __forceinline__ bf16x8 cat(s16x4 a, s16x4 b) { return __builtin_shufflevector(a, b, 0, 1, 2, 3, 4, 5, 6, 7); }
#define WAITV(n) asm volatile("s_waitcnt vmcnt(" #n ")" ::: "memory")
#define WAITL0() asm volatile("s_waitcnt lgkmcnt(0)" ::: "memory")
#define BAR() do { asm volatile("" ::: "memory"); __builtin_amdgcn_s_barrier(); asm volatile("" ::: "memory"); } while (0)
template <int N> __device__ __forceinline__ void waitv() { asm volatile("s_waitcnt vmcnt(%0)" ::"n"(N) : "memory"); }

__device__ __forceinline__ void ln_phase(const Params& p, LAS char* lds, int w) {
    const int tid = threadIdx.x, lane = tid & 63, wid = __builtin_amdgcn_readfirstlane(tid >> 6);
    const int rb = w & 63, cg = w >> 6;
    u32x4 v[4];
#pragma unroll
    for (int i = 0; i < 4; ++i) v[i] = *(const GAS u32x4*)(p.x + (32 * rb + 4 * wid + i) * DM + lane * 8);
    const int ch = 128 * cg + (tid >> 2), q = tid & 3;
    u16 c[8];
#pragma unroll
    for (int i = 0; i < 8; ++i) c[i] = *(const GAS u16*)(p.x + (32 * rb + 8 * q + i) * DM + ch);
    LAS f32x2* st = (LAS f32x2*)lds;
#pragma unroll
    for (int i = 0; i < 4; ++i) {
        float f[8];
#pragma unroll
        for (int j = 0; j < 4; ++j) { f[2 * j] = bflo(v[i][j]); f[2 * j + 1] = bfhi(v[i][j]); }
        float s = ((f[0] + f[1]) + (f[2] + f[3])) + ((f[4] + f[5]) + (f[6] + f[7]));
        const float mean = wave_sum(s) * (1.0f / 512.0f);
        float ss = 0.f;
#pragma unroll
        for (int j = 0; j < 8; ++j) { const float d = f[j] - mean; ss = fmaf(d, d, ss); }
        const float var = wave_sum(ss) * (1.0f / 512.0f);
        const float mb = rbf(mean), vb = rbf(var), sd = rbf(sqrtf(rbf(vb + 1e-5f)));
        if (lane == 0) {
            st[4 * wid + i] = f32x2{mb, 1.0f / sd};
            if (cg == 0) *(GAS u32*)(p.ws + ST_OFF + (32 * rb + 4 * wid + i) * 4) = (__float_as_uint(mb) >> 16) | (__float_as_uint(sd) & 0xffff0000u);
        }
    }
    __syncthreads();
    float o[8];
#pragma unroll
    for (int i = 0; i < 8; ++i) { const f32x2 s = st[8 * q + i]; o[i] = rbf(bf2f(c[i]) - s.x) * s.y; }
    u32x4 r = {pk2(o[0], o[1]), pk2(o[2], o[3]), pk2(o[4], o[5]), pk2(o[6], o[7])};
    GAS char* xt = (GAS char*)(p.ws + XT_OFF) + (size_t)ch * XS * 2;
    *(GAS u32x4*)(xt + (16 + 32 * rb + 8 * q) * 2) = r;
    if (rb == 0 && q < 2) *(GAS u32x4*)(xt + q * 16) = u32x4{0, 0, 0, 0};
}

constexpr int G1_STAGE = 40960, G1_DEPTH = 3;
constexpr int G1_CS = G1_STAGE * G1_DEPTH;
constexpr int G1_LDS = G1_CS + 1536;
__device__ __forceinline__ void gemm1_phase(const Params& p, LAS char* lds, int w) {
    const int tid = threadIdx.x, lane = tid & 63, wid = __builtin_amdgcn_readfirstlane(tid >> 6);
    const int xcd = w & 7, idx = w >> 3, pn = 8 * (xcd & 1) + (idx & 7), cgp = 4 * (xcd >> 1) + (idx >> 3);
    const int f0 = cgp < 11 ? 96 * cgp : 1056 + 64 * (cgp - 11), nch = cgp < 11 ? 12 : 8;
    const int rot = (pn + 2 * (cgp & 3)) & 7;
    const char* src[5]; int kstr[5];
#pragma unroll
    for (int i = 0; i < 5; ++i) {
        const int q = wid * 5 + i;
        if (q < 16) { const int row = 8 * q + (lane >> 3), c = (lane & 7) ^ (row & 7); src[i] = (const char*)(p.x + (128 * pn + row) * DM + 8 * c); kstr[i] = 128; }
        else { const int m = q >= 28, r = q - 16 - 12 * m, e = 64 * r + lane, k = e / 12; int c = (e - 12 * k) ^ (((k >> 3) & 1) << 1); if (c >= nch) c -= 8 > c ? 0 : 8;
               src[i] = (const char*)((m ? p.wu : p.wg) + k * FF + f0 + 8 * c); kstr[i] = 64 * FF * 2; }
    }
    auto issue = [&](int s) {
        const int ks = (s + rot) & 7; LAS char* dst = lds + (s % G1_DEPTH) * G1_STAGE + wid * 5120;
#pragma unroll
        for (int i = 0; i < 5; ++i) glds16(src[i] + ks * kstr[i], dst + i * 1024);
    };
    const int wm = wid & 3, wn = wid >> 2, g = lane >> 4, fr = lane & 15, qq = fr >> 2, pp = lane & 3;
    f32x4 ag[3][2], au[3][2], cs[3];
#pragma unroll
    for (int j = 0; j < 3; ++j) { cs[j] = f32x4{0, 0, 0, 0}; for (int m = 0; m < 2; ++m) { ag[j][m] = f32x4{0, 0, 0, 0}; au[j][m] = f32x4{0, 0, 0, 0}; } }
    int xo[2][2];
#pragma unroll
    for (int m = 0; m < 2; ++m) for (int ks = 0; ks < 2; ++ks) { const int row = 32 * wm + 16 * m + fr; xo[m][ks] = row * 128 + (((4 * ks + g) ^ (row & 7)) << 4); }
    int wo[3];
#pragma unroll
    for (int j = 0; j < 3; ++j) wo[j] = 16384 + (8 * g + qq) * 192 + (((3 * wn + j) ^ (g & 1)) * 32) + pp * 8;
    const bf16x8 ones = {0x3f80, 0x3f80, 0x3f80, 0x3f80, 0x3f80, 0x3f80, 0x3f80, 0x3f80};
    issue(0); issue(1);
#pragma unroll 1
    for (int s = 0; s < 8; ++s) {
        if (s < 7) waitv<5>(); else waitv<0>();
        BAR();
        if (s + 2 < 8) issue(s + 2);
        LAS char* sb = lds + (s % G1_DEPTH) * G1_STAGE;
#pragma unroll
        for (int ks = 0; ks < 2; ++ks) {
            bf16x8 xf[2], gf[3], uf[3];
#pragma unroll
            for (int m = 0; m < 2; ++m) xf[m] = *(LAS bf16x8*)(sb + xo[m][ks]);
#pragma unroll
            for (int j = 0; j < 3; ++j) {
                LAS char* a = sb + wo[j] + ks * 32 * 192;
                gf[j] = cat(trrd(a), trrd(a + 4 * 192));
                uf[j] = cat(trrd(a + 12288), trrd(a + 12288 + 4 * 192));
            }
#pragma unroll
            for (int j = 0; j < 3; ++j)
#pragma unroll
                for (int m = 0; m < 2; ++m) { ag[j][m] = mfma16(gf[j], xf[m], ag[j][m]); au[j][m] = mfma16(uf[j], xf[m], au[j][m]); }
            if ((wm & 1) == ks) {
                if (wm & 2) { for (int j = 0; j < 3; ++j) cs[j] = mfma16(uf[j], ones, cs[j]); }
                else        { for (int j = 0; j < 3; ++j) cs[j] = mfma16(gf[j], ones, cs[j]); }
            }
        }
        WAITL0();
    }
    LAS float* cst = (LAS float*)(lds + G1_CS);
    if (fr == 0) {
#pragma unroll
        for (int j = 0; j < 3; ++j) *(LAS f32x4*)(cst + ((wm & 1) * 2 + (wm >> 1)) * 96 + 16 * (3 * wn + j) + 4 * g) = cs[j];
    }
    __syncthreads();
    float mean[2], rs[2];
#pragma unroll
    for (int m = 0; m < 2; ++m) { const u32 sv = *(const GAS u32*)(p.ws + ST_OFF + (128 * pn + 32 * wm + 16 * m + fr) * 4); mean[m] = bflo(sv); rs[m] = 1.0f / bfhi(sv); }
    const int tok = 128 * pn + 32 * wm + 16 * (g & 1) + fr, rt = tok >> 6, rr = tok & 63;
#pragma unroll
    for (int j = 0; j < 3; ++j) {
        const int t = 3 * wn + j;
        const f32x4 cg = *(LAS f32x4*)(cst + 16 * t + 4 * g) + *(LAS f32x4*)(cst + 192 + 16 * t + 4 * g);
        const f32x4 cu = *(LAS f32x4*)(cst + 96 + 16 * t + 4 * g) + *(LAS f32x4*)(cst + 288 + 16 * t + 4 * g);
        u32 hp[2][2];
#pragma unroll
        for (int m = 0; m < 2; ++m) {
            float h[4];
#pragma unroll
            for (int r = 0; r < 4; ++r) {
                const float gv = rbf((ag[j][m][r] - mean[m] * cg[r]) * rs[m]), uv = rbf((au[j][m][r] - mean[m] * cu[r]) * rs[m]);
                const float z = gv * fmaf(gv * gv, -0.10294324f, -2.3022082f);
                const float ge = rbf(gv * __builtin_amdgcn_rcpf(1.0f + __builtin_amdgcn_exp2f(z)));
                h[r] = ge * uv;
            }
            hp[m][0] = pk2(h[0], h[1]); hp[m][1] = pk2(h[2], h[3]);
        }
        auto s0 = __builtin_amdgcn_permlane16_swap(hp[0][0], hp[1][0], false, false);
        auto s1 = __builtin_amdgcn_permlane16_swap(hp[0][1], hp[1][1], false, false);
        const int chunk = 2 * t + (g >> 1);
        if (chunk < nch) {
            const int f = f0 + 8 * chunk, blk = f >> 6, cc = (f >> 3) & 7;
            *(GAS u32x4*)(p.ws + H_OFF + ((size_t)(rt * 22 + blk) * 64 + rr) * 128 + ((cc ^ (rr & 7)) << 4)) = u32x4{s0[0], s1[0], s0[1], s1[1]};
        }
    }
}

constexpr int C_EF = 0;
constexpr int C_EFCH = 2 * 32 * 544;
constexpr int C_KC = 2 * C_EFCH;
constexpr int C_KR = C_KC + 2 * 1296;
constexpr int C_U  = C_KR + 2 * 4608;
constexpr int C_LDS = C_U + 8 * 4160;
struct cpx { float re, im; };
__device__ __forceinline__ cpx cmul(cpx a, cpx b) { return cpx{fmaf(a.re, b.re, -a.im * b.im), fmaf(a.re, b.im, a.im * b.re)}; }
__device__ __forceinline__ cpx cexpp(float arl, float air, float pw) {
    const float mag = __builtin_amdgcn_exp2f(arl * pw), th = __builtin_amdgcn_fractf(air * pw);
    return cpx{mag * __builtin_amdgcn_cosf(th), mag * __builtin_amdgcn_sinf(th)};
}
template <int TG> __device__ __forceinline__ void conv_body(LAS char* ub, LAS char* kb, int rh, f32x4 (&acc)[4]) {
    constexpr int T[4] = {TG ? 1 : 0, TG ? 2 : 3, TG ? 5 : 4, TG ? 6 : 7};
    constexpr int NF = T[3] + 1;
#pragma unroll 1
    for (int i = 0; i < 4; ++i) {
        const int rho = 4 * rh + i;
        bf16x8 A[NF], B[NF];
        LAS char* ua = ub + rho * 64;
        LAS char* ka = kb + rho * 64;
#pragma unroll
        for (int s = 0; s < NF; ++s) {
            const s16x4 lo = *(LAS s16x4*)(ua + s * 512), hi = *(LAS s16x4*)(ua + s * 512 + 8);
            A[s] = cat(lo, hi);
            B[s] = *(LAS bf16x8*)(ka + (7 - s) * 512);
        }
#pragma unroll
        for (int k = 0; k < 4; ++k)
#pragma unroll
            for (int s = 0; s <= T[k]; ++s) acc[k] = mfma16(A[s], B[T[k] - s], acc[k]);
    }
}
__device__ __forceinline__ void conv_phase(const Params& p, LAS char* lds, int w) {
    const int tid = threadIdx.x, lane = tid & 63, wid = __builtin_amdgcn_readfirstlane(tid >> 6);
#pragma unroll
    for (int i = 0; i < 4; ++i) {
        const int pc = wid, chn = pc >> 2, cc = pc & 3;
        const char* g = p.ws + XT_OFF + ((size_t)(2 * w + chn) * XS + cc + 1 + i * 512 + lane * 8) * 2;
        glds16(g, lds + C_U + pc * 4160 + i * 1024);
    }
    if (lane < 2) { const int chn = wid >> 2, cc = wid & 3; glds16(p.ws + XT_OFF + ((size_t)(2 * w + chn) * XS + cc + 1 + 2048 + lane * 8) * 2, lds + C_U + wid * 4160 + 4096); }
    {
        const int chn = tid >> 8, n = tid & 127, half = (tid >> 7) & 1, d = 2 * w + chn;
        const float dt = rbf(__expf(bf2f(p.logdt[d])));
        const float ar = -rbf(__expf(bf2f(p.logA[d * NS + n]))) * dt, ai = bf2f(p.aim[d * NS + n]) * dt;
        const cpx c2 = {2.0f * bf2f(p.cre[d * NS + n]) * dt, 2.0f * bf2f(p.cim[d * NS + n]) * dt};
        const float arl = ar * 1.44269504f, air = ai * 0.15915494f;
        LAS char* eb = lds + C_EF + chn * C_EFCH + n * 4; LAS char* fb = eb + 32 * 544;
        auto we = [&](int r, cpx v) { *(LAS u32*)(eb + r * 544) = pk2(v.re, -v.im); };
        auto wf = [&](int r, cpx v) { *(LAS u32*)(fb + r * 544) = pk2(v.re, v.im); };
        const cpx w8 = cexpp(arl, air, 8.f), z256 = cexpp(arl, air, 256.f);
        if (half == 0) {
            cpx f = {1.f, 0.f}; wf(0, f);
#pragma unroll
            for (int b = 1; b < 16; ++b) { f = cmul(f, w8); wf(b, f); }
            const cpx z1 = cexpp(arl, air, 1.f); cpx e = c2; we(0, e);
#pragma unroll
            for (int a = 1; a < 8; ++a) { e = cmul(e, z1); we(a, e); }
            const cpx z2 = cmul(z1, z1); e = cmul(c2, z256); we(8, e); e = cmul(e, z2); we(9, e); e = cmul(e, z2); we(10, e); e = cmul(e, z2); we(11, e);
        } else {
            cpx f = cexpp(arl, air, 128.f); wf(16, f);
#pragma unroll
            for (int b = 17; b < 32; ++b) { f = cmul(f, w8); wf(b, f); }
            const cpx z4 = cexpp(arl, air, 4.f), z512 = cexpp(arl, air, 512.f), z1024 = cexpp(arl, air, 1024.f);
            cpx e = cmul(c2, z512); we(12, e); we(13, cmul(e, z4)); e = cmul(e, z256); we(14, e); we(15, cmul(e, z4));
            e = cmul(c2, z1024); we(16, e); e = cmul(e, z256); we(17, e); e = cmul(e, z256); we(18, e); e = cmul(e, z256); we(19, e); e = cmul(e, z256); we(20, e);
        }
    }
    __syncthreads();
    {
        const int chn = wid >> 2, rt = (wid >> 1) & 1, ct = wid & 1, fr = lane & 15, g = lane >> 4;
        LAS char* ea = lds + C_EF + chn * C_EFCH + (16 * rt + fr) * 544 + g * 16;
        LAS char* fa = lds + C_EF + chn * C_EFCH + (32 + 16 * ct + fr) * 544 + g * 16;
        f32x4 a = {0, 0, 0, 0};
#pragma unroll
        for (int s = 0; s < 8; ++s) a = mfma16(*(LAS bf16x8*)(ea + s * 64), *(LAS bf16x8*)(fa + s * 64), a);
        LAS u16* kc = (LAS u16*)(lds + C_KC + chn * 1296);
        const int beta = 16 * ct + fr;
#pragma unroll
        for (int r = 0; r < 4; ++r) {
            const int al = 16 * rt + 4 * g + r; int ci;
            if (al < 8) ci = al + 8 * beta;
            else if (al < 12) ci = 256 + (al - 8) + 4 * beta;
            else if (al < 16) ci = 384 + 64 * ((al - 12) >> 1) + ((al - 12) & 1) + 2 * beta;
            else if (al < 20) ci = 512 + 32 * (al - 16) + beta;
            else ci = (al == 20 && beta == 0) ? 640 : -1;
            if (ci >= 0) kc[ci] = (u16)pk2(a[r], a[r]);
        }
    }
    __syncthreads();
    {
        const int chn = tid >> 8, i = tid & 255, m = 255 - i;
        const LAS u16* kc = (const LAS u16*)(lds + C_KC + chn * 1296);
        const int e = m >= 128 ? 3 : m >= 64 ? 2 : m >= 32 ? 1 : 0, hb = ((1 << e) >> 1) - (e ? 1 : 0);
        u32 v[8];
#pragma unroll
        for (int k = 0; k < 8; ++k) { const int j = 8 * m + 7 - k; const int q = (j + hb + (e ? ((j >> e) & 1) : 0)) >> e; v[k] = kc[q + 128 * e]; }
        *(LAS u32x4*)(lds + C_KR + chn * 4608 + i * 16) = u32x4{v[0] | (v[1] << 16), v[2] | (v[3] << 16), v[4] | (v[5] << 16), v[6] | (v[7] << 16)};
        if (i < 32) *(LAS u32x4*)(lds + C_KR + chn * 4608 + 4096 + i * 16) = u32x4{0, 0, 0, 0};
    }
    WAITV(0);
    __syncthreads();
    const int chn = wid >> 2, rh = (wid >> 1) & 1, tg = wid & 1, fr = lane & 15, g = lane >> 4;
    f32x4 acc[4] = {{0, 0, 0, 0}, {0, 0, 0, 0}, {0, 0, 0, 0}, {0, 0, 0, 0}};
    LAS char* ub = lds + C_U + (chn * 4 + (fr & 3)) * 4160 + ((fr >> 2) * 4 + 8 * g) * 2;
    LAS char* kb = lds + C_KR + chn * 4608 + (2032 - 32 * 56 - 16 * fr + 8 * g) * 2;
    if (tg == 0) conv_body<0>(ub, kb, rh, acc); else conv_body<1>(ub, kb, rh, acc);
    __syncthreads();
    LAS f32x4* ex = (LAS f32x4*)(lds + C_EF) + wid * 128;
    ex[lane] = rh ? acc[0] : acc[2]; ex[64 + lane] = rh ? acc[1] : acc[3];
    __syncthreads();
    const LAS f32x4* ey = (const LAS f32x4*)(lds + C_EF) + (wid ^ 2) * 128;
    const f32x4 r0 = (rh ? acc[2] : acc[0]) + ey[lane], r1 = (rh ? acc[3] : acc[1]) + ey[64 + lane];
    const int t0 = tg ? (rh ? 5 : 1) : (rh ? 4 : 0), t1 = tg ? (rh ? 6 : 2) : (rh ? 7 : 3);
    GAS char* yb = (GAS char*)(p.ws + YT_OFF) + (size_t)(2 * w + chn) * 4096 + fr * 32 + g * 8;
    *(GAS u32x2*)(yb + t0 * 512) = u32x2{pk2(r0[0], r0[1]), pk2(r0[2], r0[3])};
    *(GAS u32x2*)(yb + t1 * 512) = u32x2{pk2(r1[0], r1[1]), pk2(r1[2], r1[3])};
}

constexpr int G2_STAGE = 32768, G2_DEPTH = 4;
constexpr int G2_LDS = G2_STAGE * G2_DEPTH;
__device__ __forceinline__ void gemm2_phase(const Params& p, LAS char* lds, int w) {
    const int tid = threadIdx.x, lane = tid & 63, wid = __builtin_amdgcn_readfirstlane(tid >> 6);
    const int xcd = w & 7, idx = w >> 3, rt = 4 * xcd + (idx & 3), ct = idx >> 2;
    const char* src[4]; int kstr[4]; int rowk[4];
#pragma unroll
    for (int i = 0; i < 4; ++i) {
        const int q = wid * 4 + i;
        if (q < 16) { src[i] = p.ws + H_OFF + (size_t)rt * 22 * 8192 + q * 1024 + lane * 16; kstr[i] = 16384; rowk[i] = 0; }
        else { const int k = 8 * (q - 16) + (lane >> 3), cp = lane & 7, f = ((k >> 1) & 1) | (((k >> 3) & 1) << 1), c = (((cp >> 1) ^ f) << 1) | (cp & 1);
               src[i] = (const char*)(p.wd + k * DM + 64 * ct + 8 * c); kstr[i] = 128 * DM * 2; rowk[i] = 8 * (q - 16); }
    }
    auto issue = [&](int s) {
        LAS char* dst = lds + (s % G2_DEPTH) * G2_STAGE + wid * 4096;
#pragma unroll
        for (int i = 0; i < 4; ++i) {
            int ss = s; if (s == 10 && rowk[i] >= 96) ss = 9;
            glds16(src[i] + (size_t)ss * kstr[i], dst + i * 1024);
        }
    };
    const int kg = wid >> 2, wm = wid & 1, wn = (wid >> 1) & 1, g = lane >> 4, fr = lane & 15, qq = fr >> 2, pp = lane & 3;
    f32x4 acc[2][2] = {{{0, 0, 0, 0}, {0, 0, 0, 0}}, {{0, 0, 0, 0}, {0, 0, 0, 0}}};
    int ho[2][2], wo[2];
#pragma unroll
    for (int m = 0; m < 2; ++m) for (int c2 = 0; c2 < 2; ++c2) { const int row = 32 * wm + 16 * m + fr; ho[m][c2] = row * 128 + (((4 * c2 + g) ^ (row & 7)) << 4); }
    const int fk = ((qq >> 1) & 1) | ((g & 1) << 1);
#pragma unroll
    for (int n = 0; n < 2; ++n) wo[n] = 16384 + (8 * g + qq) * 128 + (((2 * wn + n) ^ fk) * 32) + pp * 8;
    issue(0); issue(1); issue(2);
#pragma unroll 1
    for (int s = 0; s < 11; ++s) {
        if (s < 9) waitv<8>(); else if (s == 9) waitv<4>(); else waitv<0>();
        BAR();
        if (s + 3 < 11) issue(s + 3);
        LAS char* sb = lds + (s % G2_DEPTH) * G2_STAGE;
#pragma unroll
        for (int i = 0; i < 2; ++i) {
            const int kk = kg + 2 * i;
            if (s == 10 && kk == 3) break;
            bf16x8 hf[2], wf[2];
#pragma unroll
            for (int m = 0; m < 2; ++m) hf[m] = *(LAS bf16x8*)(sb + i * 8192 + (kg ? ho[m][1] : ho[m][0]));
#pragma unroll
            for (int n = 0; n < 2; ++n) { LAS char* a = sb + wo[n] + kk * 32 * 128; wf[n] = cat(trrd(a), trrd(a + 512)); }
#pragma unroll
            for (int n = 0; n < 2; ++n)
#pragma unroll
                for (int m = 0; m < 2; ++m) acc[n][m] = mfma16(wf[n], hf[m], acc[n][m]);
        }
        WAITL0();
    }
    __syncthreads();
    LAS f32x4* ex = (LAS f32x4*)lds + wid * 128;
    ex[lane] = kg ? acc[0][0] : acc[0][1]; ex[64 + lane] = kg ? acc[1][0] : acc[1][1];
    __syncthreads();
    const LAS f32x4* ey = (const LAS f32x4*)lds + (wid ^ 4) * 128;
    f32x4 r[2]; r[0] = (kg ? acc[0][1] : acc[0][0]) + ey[lane]; r[1] = (kg ? acc[1][1] : acc[1][0]) + ey[64 + lane];
    const int tok = 64 * rt + 32 * wm + 16 * kg + fr;
    const u32 sv = *(const GAS u32*)(p.ws + ST_OFF + tok * 4);
    const float mean = bflo(sv), rs = 1.0f / bfhi(sv);
#pragma unroll
    for (int n = 0; n < 2; ++n) {
        const int d = 64 * ct + 32 * wn + 16 * n + 4 * g;
        const u32x2 xv = *(const GAS u32x2*)(p.x + tok * DM + d), dv = *(const GAS u32x2*)(p.dskip + d);
        float o[4];
#pragma unroll
        for (int j = 0; j < 4; ++j) {
            const float xx = (j & 1) ? bfhi(xv[j >> 1]) : bflo(xv[j >> 1]), ds = (j & 1) ? bfhi(dv[j >> 1]) : bflo(dv[j >> 1]);
            const float y = bf2f(*(const GAS u16*)(p.ws + YT_OFF + ((size_t)(d + j) * SL + tok) * 2));
            const float xn = rbf(rbf(xx - mean) * rs);
            const float a = rbf(y + rbf(xn * ds));
            o[j] = rbf(xx + a) + rbf(r[n][j]);
        }
        *(GAS u32x2*)(p.out + tok * DM + d) = u32x2{pk2(o[0], o[1]), pk2(o[2], o[3])};
    }
}

__global__ __launch_bounds__(512, 2) void k_ln(Params p) { extern __shared__ __attribute__((aligned(16))) char smem[]; ln_phase(p, (LAS char*)smem, blockIdx.x); }
__global__ __launch_bounds__(512, 2) void k_g1(Params p) { extern __shared__ __attribute__((aligned(16))) char smem[]; gemm1_phase(p, (LAS char*)smem, blockIdx.x); }
__global__ __launch_bounds__(512, 2) void k_cv(Params p) { extern __shared__ __attribute__((aligned(16))) char smem[]; conv_phase(p, (LAS char*)smem, blockIdx.x); }
__global__ __launch_bounds__(512, 2) void k_g2(Params p) { extern __shared__ __attribute__((aligned(16))) char smem[]; gemm2_phase(p, (LAS char*)smem, blockIdx.x); }

extern "C" void kernel_launch(void* const* d_in, const int* in_sizes, int n_in, void* d_out, int out_size, void* d_ws, size_t ws_size, hipStream_t stream) {
    Params p;
    p.x = (const u16*)d_in[0]; p.logA = (const u16*)d_in[1]; p.aim = (const u16*)d_in[2]; p.cre = (const u16*)d_in[3]; p.cim = (const u16*)d_in[4];
    p.logdt = (const u16*)d_in[5]; p.dskip = (const u16*)d_in[6]; p.wg = (const u16*)d_in[8]; p.wu = (const u16*)d_in[9]; p.wd = (const u16*)d_in[10];
    p.out = (u16*)d_out; p.ws = (char*)d_ws;
    (void)hipFuncSetAttribute((const void*)k_g1, hipFuncAttributeMaxDynamicSharedMemorySize, G1_LDS);
    (void)hipFuncSetAttribute((const void*)k_cv, hipFuncAttributeMaxDynamicSharedMemorySize, C_LDS);
    (void)hipFuncSetAttribute((const void*)k_g2, hipFuncAttributeMaxDynamicSharedMemorySize, G2_LDS);
    k_ln<<<256, 512, 1024, stream>>>(p);
    k_g1<<<256, 512, G1_LDS, stream>>>(p);
    k_cv<<<256, 512, C_LDS, stream>>>(p);
    k_g2<<<256, 512, G2_LDS, stream>>>(p);
}
```

```cpp
#include <hip/hip_runtime.h>
#include <hip/hip_bf16.h>

#define LAS __attribute__((address_space(3)))
#define GAS __attribute__((address_space(1)))
typedef unsigned short u16;
typedef unsigned int u32;
typedef unsigned long long u64;
typedef short bf16x8 __attribute__((ext_vector_type(8)));
typedef short s16x4 __attribute__((ext_vector_type(4)));
typedef float f32x4 __attribute__((ext_vector_type(4)));
typedef float f32x2 __attribute__((ext_vector_type(2)));
typedef unsigned u32x4 __attribute__((ext_vector_type(4)));
typedef unsigned u32x2 __attribute__((ext_vector_type(2)));
typedef __bf16 bf16x2_t __attribute__((ext_vector_type(2)));

constexpr int SL = 2048, DM = 512, NS = 128, FF = 1376;
constexpr int XS = 2080;
constexpr size_t CTL_OFF = 0;
constexpr int FA_W = 64, FH_W = 64 + 256, FY_W = 64 + 512;
constexpr size_t SG_OFF = 8192;
constexpr size_t XT_OFF = SG_OFF + 4 * 2048 * 8;
constexpr size_t H_OFF  = XT_OFF + 2134016;
constexpr size_t YT_OFF = H_OFF + (size_t)32 * 22 * 8192;
constexpr size_t WS_END = YT_OFF + (size_t)512 * 2048 * 2;
constexpr size_t DIAG_OFF = (size_t)128 << 20;
constexpr int G1_STAGE = 40960;
constexpr int L_U = 32768, L_EF = 66048, EF_CH = 53 * 544;
constexpr int L_MISC = 131584;
constexpr int L_PAR = L_MISC, L_ST = L_MISC + 4096, L_CS = L_MISC + 5120, L_KC = L_MISC + 6656, L_KR = L_MISC + 9280, L_ST2 = L_MISC + 18496, L_LN = L_MISC + 19008;
constexpr int LDS_BYTES = 163840;
static_assert(L_EF + 2 * EF_CH <= L_MISC && L_LN + 256 <= LDS_BYTES, "lds map");

struct Params {
    const u16 *x, *logA, *aim, *cre, *cim, *logdt, *dskip, *wg, *wu, *wd;
    u16* out; char* ws;
};
#define STAMP(i) do {} while (0)
#define SSTAMP(i) do {} while (0)

__device__ __forceinline__ float bf2f(u32 b) { return __uint_as_float(b << 16); }
__device__ __forceinline__ float bflo(u32 v) { return __uint_as_float(v << 16); }
__device__ __forceinline__ float bfhi(u32 v) { return __uint_as_float(v & 0xffff0000u); }
__device__ __forceinline__ u32 pk2(float a, float b) { bf16x2_t v = {(__bf16)a, (__bf16)b}; return __builtin_bit_cast(u32, v); }
__device__ __forceinline__ float rbf(float a) { return __uint_as_float(pk2(a, a) << 16); }
template <int CTRL, int RM> __device__ __forceinline__ float dpp_add(float v) {
    return v + __int_as_float(__builtin_amdgcn_update_dpp(0, __float_as_int(v), CTRL, RM, 0xf, true));
}
__device__ __forceinline__ float wave_sum(float v) {
    v = dpp_add<0x111, 0xf>(v); v = dpp_add<0x112, 0xf>(v); v = dpp_add<0x114, 0xf>(v); v = dpp_add<0x118, 0xf>(v);
    v = dpp_add<0x142, 0xa>(v); v = dpp_add<0x143, 0xc>(v);
    return __int_as_float(__builtin_amdgcn_readlane(__float_as_int(v), 63));
}
__device__ __forceinline__ f32x4 mfma16(bf16x8 a, bf16x8 b, f32x4 c) { return __builtin_amdgcn_mfma_f32_16x16x32_bf16(a, b, c, 0, 0, 0); }
template <int SC1> __device__ __forceinline__ void glds16(const void* sbase, u32 voff, u32 lds) {
    if (SC1) asm volatile("s_mov_b32 m0, %2\n\ts_nop 0\n\tglobal_load_lds_dwordx4 %0, %1 sc1" ::"v"(voff), "s"(sbase), "s"(lds) : "memory");
    else     asm volatile("s_mov_b32 m0, %2\n\ts_nop 0\n\tglobal_load_lds_dwordx4 %0, %1" ::"v"(voff), "s"(sbase), "s"(lds) : "memory");
}
__device__ __forceinline__ u32 ldsaddr(LAS char* p) { return (u32)(size_t)p; }
typedef short v4i16_t __attribute__((ext_vector_type(4)));
__device__ __forceinline__ s16x4 trrd(LAS char* p) { return __builtin_amdgcn_ds_read_tr16_b64_v4i16((LAS v4i16_t*)p); }
__device__ __forceinline__ bf16x8 cat(s16x4 a, s16x4 b) { return __builtin_shufflevector(a, b, 0, 1, 2, 3, 4, 5, 6, 7); }
#define WAITV0() asm volatile("s_waitcnt vmcnt(0)" ::: "memory")
#define WAITL0() __builtin_amdgcn_s_waitcnt(0xC07F)
#define BAR() do { asm volatile("" ::: "memory"); __builtin_amdgcn_s_barrier(); asm volatile("" ::: "memory"); } while (0)
#define LBAR() do { WAITL0(); BAR(); } while (0)
#define SB0() __builtin_amdgcn_sched_barrier(0)
#define SGB(mask, n) __builtin_amdgcn_sched_group_barrier(mask, n, 0)
template <int N> __device__ __forceinline__ void waitv() { asm volatile("s_waitcnt vmcnt(%0)" ::"n"(N) : "memory"); }
#define RLX_AGENT __ATOMIC_RELAXED, __HIP_MEMORY_SCOPE_AGENT
__device__ __forceinline__ u64 ld_u64(const char* p) { return __hip_atomic_load((GAS u64*)p, RLX_AGENT); }
__device__ __forceinline__ void st_u64(char* p, u64 v) { __hip_atomic_store((GAS u64*)p, v, RLX_AGENT); }
__device__ __forceinline__ void st16_sc1(__amdgpu_buffer_rsrc_t rs, u32 off, u32x4 v) { __builtin_amdgcn_raw_buffer_store_b128(v, rs, off, 0, 16); }
__device__ __forceinline__ void st8_sc1(__amdgpu_buffer_rsrc_t rs, u32 off, u32x2 v) { __builtin_amdgcn_raw_buffer_store_b64(v, rs, off, 0, 16); }
constexpr u32 SPIN_CAP = 1u << 16;
__device__ __forceinline__ void poll_flags(const char* f, int n, u64 tag, int lane) {
    for (u32 sp = 0; sp < SPIN_CAP; ++sp) {
        const bool ok = lane >= n || ld_u64(f + lane * 8) == tag;
        if (__all(ok)) break;
        __builtin_amdgcn_s_sleep(2);
    }
    __builtin_amdgcn_fence(__ATOMIC_ACQUIRE, "wavefront");
}
template <int NR> __device__ __forceinline__ void poll_stats(const char* g, u32 tag, LAS f32x2* dst, int lane) {
    u64 v[NR];
    for (u32 sp = 0; sp < SPIN_CAP; ++sp) {
        bool ok = true;
#pragma unroll
        for (int i = 0; i < NR; ++i) { v[i] = ld_u64(g + (lane + 64 * i) * 8); ok &= (u32)(v[i] >> 32) == tag; }
        if (__all(ok)) break;
        __builtin_amdgcn_s_sleep(2);
    }
#pragma unroll
    for (int i = 0; i < NR; ++i) dst[lane + 64 * i] = f32x2{bflo((u32)v[i]), 1.0f / bfhi((u32)v[i])};
}

struct G1Frag { bf16x8 x[2], f1[3], f2[3]; };
__device__ __forceinline__ void g1_read(G1Frag& f, LAS char* xb, LAS char* w1a, LAS char* w1b, LAS char* w2a, LAS char* w2b) {
    f.x[0] = *(LAS bf16x8*)(xb); f.x[1] = *(LAS bf16x8*)(xb + 2048);
    f.f1[0] = cat(trrd(w1a), trrd(w1a + 768)); f.f2[0] = cat(trrd(w2a), trrd(w2a + 768));
    f.f1[1] = cat(trrd(w1b), trrd(w1b + 768)); f.f2[1] = cat(trrd(w2b), trrd(w2b + 768));
    f.f1[2] = cat(trrd(w1a + 64), trrd(w1a + 64 + 768)); f.f2[2] = cat(trrd(w2a + 64), trrd(w2a + 64 + 768));
}
template <bool CS> __device__ __forceinline__ void g1_mma(const G1Frag& f, f32x4 (&a1)[3][2], f32x4 (&a2)[3][2], f32x4 (&cs)[3], bf16x8 ones) {
#pragma unroll
    for (int j = 0; j < 3; ++j) {
#pragma unroll
        for (int m = 0; m < 2; ++m) { a1[j][m] = mfma16(f.f1[j], f.x[m], a1[j][m]); a2[j][m] = mfma16(f.f2[j], f.x[m], a2[j][m]); }
        if (CS) cs[j] = mfma16(f.f1[j], ones, cs[j]);
    }
}
template <int NM> __device__ __forceinline__ void sched_g1() {
#pragma unroll
    for (int i = 0; i < 12; ++i) { SGB(0x8, 1); SGB(0x100, 1); if (i < 2) SGB(0x100, 1); }
    if (NM > 12) SGB(0x8, NM - 12);
}
struct cpx { float re, im; };
__device__ __forceinline__ cpx cmul(cpx a, cpx b) { return cpx{fmaf(a.re, b.re, -a.im * b.im), fmaf(a.re, b.im, a.im * b.re)}; }
__device__ __forceinline__ cpx cexpp(float arl, float air, float pw) {
    const float mag = __builtin_amdgcn_exp2f(arl * pw), th = __builtin_amdgcn_fractf(air * pw);
    return cpx{mag * __builtin_amdgcn_cosf(th), mag * __builtin_amdgcn_sinf(th)};
}
template <int TG> __device__ __forceinline__ void conv_rho(LAS char* ua, LAS char* ka, f32x4 (&acc)[4]) {
    constexpr int T[4] = {TG ? 1 : 0, TG ? 2 : 3, TG ? 5 : 4, TG ? 6 : 7};
    constexpr int NF = T[3] + 1;
    bf16x8 A[NF], B[NF];
#pragma unroll
    for (int s = 0; s < NF; ++s) {
        const s16x4 lo = *(LAS s16x4*)(ua + s * 512), hi = *(LAS s16x4*)(ua + s * 512 + 8);
        A[s] = cat(lo, hi);
        B[s] = *(LAS bf16x8*)(ka + (7 - s) * 512);
    }
#pragma unroll
    for (int k = 0; k < 4; ++k)
#pragma unroll
        for (int s = 0; s <= T[k]; ++s) acc[k] = mfma16(A[s], B[T[k] - s], acc[k]);
}
struct G2Frag { bf16x8 h[2], w[2]; };
__device__ __forceinline__ void g2_read(G2Frag& f, LAS char* hb, LAS char* w0, LAS char* w1) {
    f.h[0] = *(LAS bf16x8*)(hb); f.h[1] = *(LAS bf16x8*)(hb + 2048);
    f.w[0] = cat(trrd(w0), trrd(w0 + 512)); f.w[1] = cat(trrd(w1), trrd(w1 + 512));
}
__device__ __forceinline__ void g2_mma(const G2Frag& f, f32x4 (&acc)[2][2]) {
#pragma unroll
    for (int n = 0; n < 2; ++n)
#pragma unroll
        for (int m = 0; m < 2; ++m) acc[n][m] = mfma16(f.w[n], f.h[m], acc[n][m]);
}
__device__ __forceinline__ int g2_slot(int s) { const int q = s & 3; return q == 0 ? 0 : q == 1 ? L_EF : q == 2 ? L_EF + 32768 : L_U; }

__device__ __forceinline__ void math_waves(const Params& p, LAS char* lds, const int w) {
    const int tid = threadIdx.x, lane = tid & 63, wid = __builtin_amdgcn_readfirstlane(tid >> 6);
    const int g = lane >> 4, fr = lane & 15, qq = fr >> 2, pp = lane & 3;
    const __amdgpu_buffer_rsrc_t wsr = __builtin_amdgcn_make_buffer_rsrc(p.ws, 0, (int)WS_END, 0x00020000);
    STAMP(0);
    {
        const int rb = w & 63, cq = w >> 6;
        const u64 ep = ld_u64(p.ws + CTL_OFF);
        u32x4 v[4];
#pragma unroll
        for (int i = 0; i < 4; ++i) v[i] = *(const GAS u32x4*)(p.x + (32 * rb + 4 * wid + i) * DM + lane * 8);
        const int ch = 128 * cq + (tid >> 2), q = tid & 3;
        u16 c[8];
#pragma unroll
        for (int i = 0; i < 8; ++i) c[i] = *(const GAS u16*)(p.x + (32 * rb + 8 * q + i) * DM + ch);
        LAS f32x2* st = (LAS f32x2*)(lds + L_LN);
        u32 sv = 0;
#pragma unroll
        for (int i = 0; i < 4; ++i) {
            float f[8];
#pragma unroll
            for (int j = 0; j < 4; ++j) { f[2 * j] = bflo(v[i][j]); f[2 * j + 1] = bfhi(v[i][j]); }
            float s = ((f[0] + f[1]) + (f[2] + f[3])) + ((f[4] + f[5]) + (f[6] + f[7]));
            const float mean = wave_sum(s) * (1.0f / 512.0f);
            float ss = 0.f;
#pragma unroll
            for (int j = 0; j < 8; ++j) { const float d = f[j] - mean; ss = fmaf(d, d, ss); }
            const float var = wave_sum(ss) * (1.0f / 512.0f);
            const float mb = rbf(mean), vb = rbf(var), sd = rbf(sqrtf(rbf(vb + 1e-5f)));
            if (lane == i) { st[4 * wid + i] = f32x2{mb, 1.0f / sd}; sv = (__float_as_uint(mb) >> 16) | (__float_as_uint(sd) & 0xffff0000u); }
        }
        if (lane < 4) st_u64(p.ws + SG_OFF + (cq * 2048 + 32 * rb + 4 * wid + lane) * 8, ((u64)(u32)(ep + 1) << 32) | sv);
        LBAR();
        float o[8];
#pragma unroll
        for (int i = 0; i < 8; ++i) { const f32x2 s = st[8 * q + i]; o[i] = rbf(bf2f(c[i]) - s.x) * s.y; }
        st16_sc1(wsr, (u32)(XT_OFF + ((size_t)ch * XS + 16 + 32 * rb + 8 * q) * 2), u32x4{pk2(o[0], o[1]), pk2(o[2], o[3]), pk2(o[4], o[5]), pk2(o[6], o[7])});
    }
    STAMP(1);
    const int xcd = w & 7, idx = w >> 3, pn = 8 * (xcd & 1) + (idx & 7), cgp = 4 * (xcd >> 1) + (idx >> 3);
    const int f0 = cgp < 11 ? 96 * cgp : 1056 + 64 * (cgp - 11), nch = cgp < 11 ? 12 : 8;
    {
        const int wm = wid & 3, wn = wid >> 2, msel = wm >> 1, ksel = wm & 1;
        f32x4 a1[3][2], a2[3][2], cs[3];
#pragma unroll
        for (int j = 0; j < 3; ++j) { cs[j] = f32x4{0, 0, 0, 0}; for (int m = 0; m < 2; ++m) { a1[j][m] = f32x4{0, 0, 0, 0}; a2[j][m] = f32x4{0, 0, 0, 0}; } }
        int xo[2], wa[2][2], wb[2][2];
#pragma unroll
        for (int o = 0; o < 2; ++o) {
            const int row = 32 * wm + fr, ks = o ^ ksel; xo[o] = row * 128 + (((4 * ks + g) ^ (row & 7)) << 4);
#pragma unroll
            for (int im = 0; im < 2; ++im) {
                const int base = 16384 + ((im ^ msel) * 12288) + (ks * 32 + 8 * g + qq) * 192 + pp * 8;
                wa[o][im] = base + (((3 * wn) ^ (g & 1)) * 32);
                wb[o][im] = base + (((3 * wn + 1) ^ (g & 1)) * 32);
            }
        }
        const bf16x8 ones = {0x3f80, 0x3f80, 0x3f80, 0x3f80, 0x3f80, 0x3f80, 0x3f80, 0x3f80};
        BAR();
        STAMP(2);
        G1Frag A, B;
        g1_read(A, lds + xo[0], lds + wa[0][0], lds + wb[0][0], lds + wa[0][1], lds + wb[0][1]);
        int so = 0, slot = 0;
#pragma unroll 1
        for (int s = 0; s < 8; ++s) {
            LAS char* sb = lds + so;
            g1_read(B, sb + xo[1], sb + wa[1][0], sb + wb[1][0], sb + wa[1][1], sb + wb[1][1]);
            g1_mma<true>(A, a1, a2, cs, ones);
            sched_g1<15>();
            SB0();
            slot = slot == 2 ? 0 : slot + 1;
            so = slot * G1_STAGE;
            WAITL0();
            if (s < 7) { if (s == 3) WAITV0(); BAR(); }
            SB0();
            sb = lds + so;
            g1_read(A, sb + xo[0], sb + wa[0][0], sb + wb[0][0], sb + wa[0][1], sb + wb[0][1]);
            g1_mma<false>(B, a1, a2, cs, ones);
            sched_g1<12>();
            SB0();
        }
        STAMP(3);
        LAS float* cst = (LAS float*)(lds + L_CS);
        if (fr == 0) {
#pragma unroll
            for (int j = 0; j < 3; ++j) *(LAS f32x4*)(cst + (ksel * 2 + msel) * 96 + 16 * (3 * wn + j) + 4 * g) = cs[j];
        }
        LBAR();
        f32x2 st[2];
#pragma unroll
        for (int m = 0; m < 2; ++m) st[m] = *(LAS f32x2*)(lds + L_ST + (32 * wm + 16 * m + fr) * 8);
        const int tok = 128 * pn + 32 * wm + 16 * (g & 1) + fr, rt = tok >> 6, rr = tok & 63;
#pragma unroll
        for (int j = 0; j < 3; ++j) {
            const int t = 3 * wn + j;
            const f32x4 cg = *(LAS f32x4*)(cst + 16 * t + 4 * g) + *(LAS f32x4*)(cst + 192 + 16 * t + 4 * g);
            const f32x4 cu = *(LAS f32x4*)(cst + 96 + 16 * t + 4 * g) + *(LAS f32x4*)(cst + 288 + 16 * t + 4 * g);
            u32 hp[2][2];
#pragma unroll
            for (int m = 0; m < 2; ++m) {
                float h[4];
#pragma unroll
                for (int r = 0; r < 4; ++r) {
                    const float ga = msel ? a2[j][m][r] : a1[j][m][r], ua = msel ? a1[j][m][r] : a2[j][m][r];
                    const float gv = rbf((ga - st[m].x * cg[r]) * st[m].y), uv = rbf((ua - st[m].x * cu[r]) * st[m].y);
                    const float z = gv * fmaf(gv * gv, -0.10294324f, -2.3022082f);
                    const float ge = rbf(gv * __builtin_amdgcn_rcpf(1.0f + __builtin_amdgcn_exp2f(z)));
                    h[r] = ge * uv;
                }
                hp[m][0] = pk2(h[0], h[1]); hp[m][1] = pk2(h[2], h[3]);
            }
            auto s0 = __builtin_amdgcn_permlane16_swap(hp[0][0], hp[1][0], false, false);
            auto s1 = __builtin_amdgcn_permlane16_swap(hp[0][1], hp[1][1], false, false);
            const int chunk = 2 * t + (g >> 1);
            if (chunk < nch) {
                const int f = f0 + 8 * chunk, blk = f >> 6, cc = (f >> 3) & 7;
                st16_sc1(wsr, (u32)(H_OFF + ((size_t)(rt * 22 + blk) * 64 + rr) * 128 + ((cc ^ (rr & 7)) << 4)), u32x4{s0[0], s1[0], s0[1], s1[1]});
            }
        }
    }
    STAMP(4);
    {
        const int chn = tid >> 8, n = tid & 127, half = (tid >> 7) & 1;
        const f32x4 pr = *(LAS f32x4*)(lds + L_PAR + (chn * 128 + n) * 16);
        const float arl = pr[0], air = pr[1]; const cpx c2 = {pr[2], pr[3]};
        LAS char* eb = lds + L_EF + chn * EF_CH + n * 4; LAS char* fb = eb + 21 * 544;
        auto we = [&](int r, cpx v) { *(LAS u32*)(eb + r * 544) = pk2(v.re, -v.im); };
        auto wf = [&](int r, cpx v) { *(LAS u32*)(fb + r * 544) = pk2(v.re, v.im); };
        const cpx w8 = cexpp(arl, air, 8.f), z256 = cexpp(arl, air, 256.f);
        if (half == 0) {
            cpx f = {1.f, 0.f}; wf(0, f);
#pragma unroll
            for (int b = 1; b < 16; ++b) { f = cmul(f, w8); wf(b, f); }
            const cpx z1 = cexpp(arl, air, 1.f); cpx e = c2; we(0, e);
#pragma unroll
            for (int a = 1; a < 8; ++a) { e = cmul(e, z1); we(a, e); }
            const cpx z2 = cmul(z1, z1); e = cmul(c2, z256); we(8, e); e = cmul(e, z2); we(9, e); e = cmul(e, z2); we(10, e); e = cmul(e, z2); we(11, e);
        } else {
            cpx f = cexpp(arl, air, 128.f); wf(16, f);
#pragma unroll
            for (int b = 17; b < 32; ++b) { f = cmul(f, w8); wf(b, f); }
            const cpx z4 = cexpp(arl, air, 4.f), z512 = cexpp(arl, air, 512.f), z1024 = cexpp(arl, air, 1024.f);
            cpx e = cmul(c2, z512); we(12, e); we(13, cmul(e, z4)); e = cmul(e, z256); we(14, e); we(15, cmul(e, z4));
            e = cmul(c2, z1024); we(16, e); e = cmul(e, z256); we(17, e); e = cmul(e, z256); we(18, e); e = cmul(e, z256); we(19, e); e = cmul(e, z256); we(20, e);
        }
    }
    LBAR();
    STAMP(5);
    {
        const int chn = wid >> 2, rt = (wid >> 1) & 1, ct = wid & 1;
        const int er = min(16 * rt + fr, 20);
        LAS char* ea = lds + L_EF + chn * EF_CH + er * 544 + g * 16;
        LAS char* fa = lds + L_EF + chn * EF_CH + (21 + 16 * ct + fr) * 544 + g * 16;
        f32x4 a = {0, 0, 0, 0};
#pragma unroll
        for (int s = 0; s < 8; ++s) a = mfma16(*(LAS bf16x8*)(ea + s * 64), *(LAS bf16x8*)(fa + s * 64), a);
        LAS u16* kc = (LAS u16*)(lds + L_KC + chn * 1296);
        const int beta = 16 * ct + fr;
#pragma unroll
        for (int r = 0; r < 4; ++r) {
            const int al = 16 * rt + 4 * g + r; int ci;
            if (al < 8) ci = al + 8 * beta;
            else if (al < 12) ci = 256 + (al - 8) + 4 * beta;
            else if (al < 16) ci = 384 + 64 * ((al - 12) >> 1) + ((al - 12) & 1) + 2 * beta;
            else if (al < 20) ci = 512 + 32 * (al - 16) + beta;
            else ci = (al == 20 && beta == 0) ? 640 : -1;
            if (ci >= 0) kc[ci] = (u16)pk2(a[r], a[r]);
        }
    }
    LBAR();
    STAMP(6);
    {
        const int chn = tid >> 8, i = tid & 255, m = 255 - i;
        const LAS u16* kc = (const LAS u16*)(lds + L_KC + chn * 1296);
        const int e = m >= 128 ? 3 : m >= 64 ? 2 : m >= 32 ? 1 : 0, hb = ((1 << e) >> 1) - (e ? 1 : 0);
        u32 v[8];
#pragma unroll
        for (int k = 0; k < 8; ++k) { const int j = 8 * m + 7 - k; const int q = (j + hb + (e ? ((j >> e) & 1) : 0)) >> e; v[k] = kc[q + 128 * e]; }
        *(LAS u32x4*)(lds + L_KR + chn * 4608 + i * 16) = u32x4{v[0] | (v[1] << 16), v[2] | (v[3] << 16), v[4] | (v[5] << 16), v[6] | (v[7] << 16)};
        if (i < 32) *(LAS u32x4*)(lds + L_KR + chn * 4608 + 4096 + i * 16) = u32x4{0, 0, 0, 0};
    }
    WAITV0();
    LBAR();
    STAMP(7);
    {
        const int chn = wid & 1, tg = (wid >> 1) & 1, rh = wid >> 2;
        f32x4 acc[4] = {{0, 0, 0, 0}, {0, 0, 0, 0}, {0, 0, 0, 0}, {0, 0, 0, 0}};
        LAS char* ub = lds + L_U + (chn * 4 + (fr & 3)) * 4160 + ((fr >> 2) * 4 + 8 * g) * 2 + rh * 256;
        LAS char* kb = lds + L_KR + chn * 4608 + (240 - 16 * fr + 8 * g) * 2 + rh * 256;
#pragma unroll 1
        for (int i = 0; i < 4; ++i) {
            if (tg == 0) conv_rho<0>(ub + i * 64, kb + i * 64, acc); else conv_rho<1>(ub + i * 64, kb + i * 64, acc);
            if (i == 1) BAR();
        }
        STAMP(8);
        LBAR();
        LAS f32x4* ex = (LAS f32x4*)(lds + L_U) + (wid & 3) * 256;
        if (rh == 0) { ex[lane] = acc[0]; ex[64 + lane] = acc[1]; ex[128 + lane] = acc[2]; ex[192 + lane] = acc[3]; }
        LBAR();
        if (rh == 1) {
            const u32 yb = (u32)(YT_OFF + (size_t)(2 * w + chn) * 4096) + fr * 32 + g * 8;
#pragma unroll
            for (int k = 0; k < 4; ++k) {
                const f32x4 r = acc[k] + ex[64 * k + lane];
                const int t = tg ? (k == 0 ? 1 : k == 1 ? 2 : k == 2 ? 5 : 6) : (k == 0 ? 0 : k == 1 ? 3 : k == 2 ? 4 : 7);
                st8_sc1(wsr, yb + t * 512, u32x2{pk2(r[0], r[1]), pk2(r[2], r[3])});
            }
        }
    }
    STAMP(9);
    {
        const int rt = 4 * xcd + (idx & 3), ct = idx >> 2;
        const int kg = wid >> 2, wm = wid & 1, wn = (wid >> 1) & 1;
        f32x4 acc[2][2] = {{{0, 0, 0, 0}, {0, 0, 0, 0}}, {{0, 0, 0, 0}, {0, 0, 0, 0}}};
        const int row = 32 * wm + fr;
        const int ho = row * 128 + (((4 * kg + g) ^ (row & 7)) << 4);
        const int fk = ((qq >> 1) & 1) | ((g & 1) << 1);
        const int wo0 = 16384 + (32 * kg + 8 * g + qq) * 128 + (((2 * wn) ^ fk) * 32) + pp * 8;
        const int wo1 = 16384 + (32 * kg + 8 * g + qq) * 128 + (((2 * wn + 1) ^ fk) * 32) + pp * 8;
        LBAR();
        STAMP(10);
        G2Frag A, B;
        g2_read(A, lds + ho, lds + wo0, lds + wo1);
#pragma unroll 1
        for (int s = 0; s < 11; ++s) {
            LAS char* sb = lds + g2_slot(s);
            g2_read(B, sb + 8192 + ho, sb + 8192 + wo0, sb + 8192 + wo1);
            g2_mma(A, acc);
            SGB(0x8, 1); SGB(0x100, 2); SGB(0x8, 1); SGB(0x100, 2); SGB(0x8, 1); SGB(0x100, 2); SGB(0x8, 1);
            SB0();
            WAITL0();
            if (s < 10) { if (s == 2) WAITV0(); BAR(); }
            SB0();
            sb = lds + g2_slot(s + 1);
            g2_read(A, sb + ho, sb + wo0, sb + wo1);
            if (s < 10 || kg == 0) g2_mma(B, acc);
            SB0();
        }
        STAMP(11);
        LAS f32x4* ex = (LAS f32x4*)(lds + L_EF) + wid * 128;
        WAITL0();
        BAR();
        ex[lane] = kg ? acc[0][0] : acc[0][1]; ex[64 + lane] = kg ? acc[1][0] : acc[1][1];
        LBAR();
        const LAS f32x4* ey = (const LAS f32x4*)(lds + L_EF) + (wid ^ 4) * 128;
        f32x4 r[2]; r[0] = (kg ? acc[0][1] : acc[0][0]) + ey[lane]; r[1] = (kg ? acc[1][1] : acc[1][0]) + ey[64 + lane];
        const int tl = 32 * wm + 16 * kg + fr;
        const f32x2 st = *(LAS f32x2*)(lds + L_ST2 + tl * 8);
#pragma unroll
        for (int n = 0; n < 2; ++n) {
            const int dl = 32 * wn + 16 * n + 4 * g, d = 64 * ct + dl;
            const u32x2 xv = *(LAS u32x2*)(lds + L_U + tl * 128 + (((dl >> 3) ^ (tl & 7)) << 4) + (dl & 4) * 2), dv = *(const GAS u32x2*)(p.dskip + d);
            float o[4];
#pragma unroll
            for (int j = 0; j < 4; ++j) {
                const float xx = (j & 1) ? bfhi(xv[j >> 1]) : bflo(xv[j >> 1]), ds = (j & 1) ? bfhi(dv[j >> 1]) : bflo(dv[j >> 1]);
                const float y = bf2f(*(LAS u16*)(lds + L_U + 8192 + (dl + j) * 128 + tl * 2));
                const float xn = rbf(rbf(xx - st.x) * st.y);
                const float a = rbf(y + rbf(xn * ds));
                o[j] = rbf(xx + a) + rbf(r[n][j]);
            }
            *(GAS u32x2*)(p.out + (64 * rt + tl) * DM + d) = u32x2{pk2(o[0], o[1]), pk2(o[2], o[3])};
        }
    }
    STAMP(12);
}

__device__ __forceinline__ void dma_waves(const Params& p, LAS char* lds, const int w, const int si) {
    const int lane = threadIdx.x & 63;
    const int xcd = w & 7, idx = w >> 3, pn = 8 * (xcd & 1) + (idx & 7), cgp = 4 * (xcd >> 1) + (idx >> 3);
    const int f0 = cgp < 11 ? 96 * cgp : 1056 + 64 * (cgp - 11), nch = cgp < 11 ? 12 : 8;
    const u32 lb = ldsaddr(lds);
    {
        const u32 vx = (lane >> 3) * 1024 + 16 * ((lane & 7) ^ (lane >> 3));
        u32 vw[12];
#pragma unroll
        for (int i = 0; i < 12; ++i) { const int e = 64 * i + lane, k = e / 12; int c = (e - 12 * k) ^ (((k >> 3) & 1) << 1); if (c >= nch) c -= 8; vw[i] = k * (FF * 2) + 16 * c; }
        const char* xs = (const char*)(p.x + 128 * pn * DM); const char* gs = (const char*)(p.wg + f0); const char* us = (const char*)(p.wu + f0);
        auto issue = [&](int s, int slot) {
            const u32 dst = lb + slot * G1_STAGE;
            if (si == 0) {
#pragma unroll
                for (int i = 0; i < 16; ++i) glds16<0>(xs + s * 128 + i * 8192, vx, dst + i * 1024);
#pragma unroll
                for (int i = 0; i < 4; ++i) glds16<0>(gs + s * (64 * FF * 2), vw[i], dst + 16384 + i * 1024);
            } else {
#pragma unroll
                for (int i = 4; i < 12; ++i) glds16<0>(gs + s * (64 * FF * 2), vw[i], dst + 16384 + i * 1024);
#pragma unroll
                for (int i = 0; i < 12; ++i) glds16<0>(us + s * (64 * FF * 2), vw[i], dst + 28672 + i * 1024);
            }
        };
        issue(0, 0); issue(1, 1);
        BAR();
        issue(2, 2);
        waitv<40>();
        BAR();
        int slot = 0;
#pragma unroll 1
        for (int s = 0; s < 7; ++s) {
            if (s < 6) waitv<20>(); else waitv<0>();
            BAR();
            if (s < 5) issue(s + 3, slot);
            slot = slot == 2 ? 0 : slot + 1;
        }
    }
    BAR();
    const int rt = 4 * xcd + (idx & 3), ct = idx >> 2;
    const u32 vl = lane * 16;
    {
        const char* g = p.ws + XT_OFF + ((size_t)(2 * w + si) * XS + 1) * 2;
#pragma unroll
        for (int cc = 0; cc < 4; ++cc) {
            const u32 dst = lb + L_U + (si * 4 + cc) * 4160;
#pragma unroll
            for (int i = 0; i < 4; ++i) glds16<1>(g + cc * 2 + i * 1024, vl, dst + i * 1024);
            if (lane < 2) glds16<1>(g + cc * 2 + 4096, vl, dst + 4096);
        }
    }
    u32 vw2; { const int k = lane >> 3, cp = lane & 7, f = (k >> 1) & 1; vw2 = k * (DM * 2) + 16 * ((((cp >> 1) ^ f) << 1) | (cp & 1)); }
    const char* wds = (const char*)(p.wd + 64 * ct); const char* hs = p.ws + H_OFF + (size_t)rt * 22 * 8192;
    auto issue2 = [&](int s) {
        const u32 dst = lb + g2_slot(s);
        if (si == 0) {
#pragma unroll
            for (int i = 0; i < 16; ++i) { const int ss = (s == 10 && i >= 12) ? 9 : s;
                glds16<0>(wds + (size_t)(ss * 128 + 8 * i) * (DM * 2), (i & 1) ? (vw2 ^ 64u) : vw2, dst + 16384 + i * 1024); }
        } else {
#pragma unroll
            for (int i = 0; i < 16; ++i) glds16<1>(hs + s * 16384 + i * 1024, vl, dst + i * 1024);
        }
    };
    if (si == 0) issue2(0);
    BAR();
    BAR();
    if (si == 0) { issue2(1); issue2(2); waitv<48>(); } else WAITV0();
    {
        if (lane < 15) {
#pragma unroll
            for (int cc = 0; cc < 4; ++cc) if (lane < 15 - cc) *(LAS u16*)(lds + L_U + (si * 4 + cc) * 4160 + lane * 2) = 0;
        }
    }
    LBAR();
    BAR();
    if (si == 1) { issue2(0); issue2(1); issue2(2); }
    BAR();
    BAR();
    waitv<32>();
    BAR();
    issue2(3);
#pragma unroll 1
    for (int s = 0; s < 10; ++s) {
        if (s < 8) waitv<32>(); else if (s == 8) waitv<24>(); else waitv<8>();
        BAR();
        if (s < 7) issue2(s + 4);
        if (s == 7) {
            if (si == 0) {
                const char* xs = (const char*)(p.x + (64 * rt) * DM + 64 * ct); const u32 vx = (lane >> 3) * 1024 + 16 * ((lane & 7) ^ (lane >> 3));
#pragma unroll
                for (int i = 0; i < 8; ++i) glds16<0>(xs + i * 8192, vx, lb + L_U + i * 1024);
            } else {
                const char* ys = p.ws + YT_OFF + ((size_t)(64 * ct) * SL + 64 * rt) * 2; const u32 vy = (lane >> 3) * 4096 + 16 * (lane & 7);
#pragma unroll
                for (int i = 0; i < 8; ++i) glds16<1>(ys + i * 32768, vy, lb + L_U + 8192 + i * 1024);
            }
        }
    }
    WAITV0();
    BAR();
    BAR();
}
__device__ __forceinline__ void io_wave(const Params& p, LAS char* lds, const int w) {
    const int lane = threadIdx.x & 63;
    const int xcd = w & 7, idx = w >> 3, pn = 8 * (xcd & 1) + (idx & 7), cgp = 4 * (xcd >> 1) + (idx >> 3);
    const int rt = 4 * xcd + (idx & 3), ct = idx >> 2;
    char* ctl = p.ws + CTL_OFF;
    const u64 tag = ld_u64(ctl) + 1;
    BAR();
    BAR();
#pragma unroll 1
    for (int s = 0; s < 7; ++s) {
        if (s == 5) poll_stats<2>(p.ws + SG_OFF + ((w & 3) * 2048 + 128 * pn) * 8, (u32)tag, (LAS f32x2*)(lds + L_ST), lane);
        if (s == 6) poll_flags(ctl + (FA_W + 64 * (w >> 6)) * 8, 64, tag, lane);
        LBAR();
        if (s == 3 && lane == 0) st_u64(ctl + (FA_W + w) * 8, tag);
    }
    BAR();
    BAR();
    BAR();
    BAR();
    if (lane == 0) st_u64(ctl + (FH_W + pn * 16 + cgp) * 8, tag);
    poll_flags(ctl + (FH_W + (rt >> 1) * 16) * 8, 16, tag, lane);
    BAR();
    BAR();
    BAR();
    BAR();
#pragma unroll 1
    for (int s = 0; s < 10; ++s) {
        if (s == 7) {
            poll_flags(ctl + (FY_W + 32 * ct) * 8, 32, tag, lane);
            poll_stats<1>(p.ws + SG_OFF + ((w & 3) * 2048 + 64 * rt) * 8, (u32)tag, (LAS f32x2*)(lds + L_ST2), lane);
        }
        LBAR();
        if (s == 2 && lane == 0) st_u64(ctl + (FY_W + w) * 8, tag);
        if (s == 8 && w == 0) {
#pragma unroll
            for (int i = 0; i < 4; ++i) poll_flags(ctl + (FA_W + 64 * i) * 8, 64, tag, lane);
            if (lane == 0) st_u64(ctl, tag);
        }
    }
    BAR();
    BAR();
}
__device__ __forceinline__ void par_wave(const Params& p, LAS char* lds, const int w) {
    const int lane = threadIdx.x & 63;
    f32x4 pr[4];
#pragma unroll
    for (int i = 0; i < 4; ++i) {
        const int chn = i >> 1, n = (i & 1) * 64 + lane, d = 2 * w + chn;
        const float dt = rbf(__expf(bf2f(p.logdt[d])));
        const float ar = -rbf(__expf(bf2f(p.logA[d * NS + n]))) * dt, ai = bf2f(p.aim[d * NS + n]) * dt;
        pr[i] = f32x4{ar * 1.44269504f, ai * 0.15915494f, 2.0f * bf2f(p.cre[d * NS + n]) * dt, 2.0f * bf2f(p.cim[d * NS + n]) * dt};
    }
    BAR();
    BAR();
#pragma unroll
    for (int i = 0; i < 4; ++i) *(LAS f32x4*)(lds + L_PAR + (i * 64 + lane) * 16) = pr[i];
#pragma unroll 1
    for (int s = 0; s < 7; ++s) LBAR();
#pragma unroll 1
    for (int s = 0; s < 20; ++s) BAR();
}

__global__ __launch_bounds__(768) void k_fused(Params p) {
    extern __shared__ __attribute__((aligned(16))) char smem[];
    LAS char* lds = (LAS char*)smem;
    const int wid = __builtin_amdgcn_readfirstlane(threadIdx.x >> 6), w = blockIdx.x;
    if (wid < 8) math_waves(p, lds, w);
    else if (wid < 10) dma_waves(p, lds, w, wid - 8);
    else if (wid == 10) io_wave(p, lds, w);
    else par_wave(p, lds, w);
}

extern "C" void kernel_launch(void* const* d_in, const int* in_sizes, int n_in, void* d_out, int out_size, void* d_ws, size_t ws_size, hipStream_t stream) {
    Params p;
    p.x = (const u16*)d_in[0]; p.logA = (const u16*)d_in[1]; p.aim = (const u16*)d_in[2]; p.cre = (const u16*)d_in[3]; p.cim = (const u16*)d_in[4];
    p.logdt = (const u16*)d_in[5]; p.dskip = (const u16*)d_in[6]; p.wg = (const u16*)d_in[8]; p.wu = (const u16*)d_in[9]; p.wd = (const u16*)d_in[10];
    p.out = (u16*)d_out; p.ws = (char*)d_ws;
    (void)hipFuncSetAttribute((const void*)k_fused, hipFuncAttributeMaxDynamicSharedMemorySize, LDS_BYTES);
    k_fused<<<256, 768, LDS_BYTES, stream>>>(p);
}
```

```cpp
#include <hip/hip_runtime.h>
#include <hip/hip_bf16.h>

#define LAS __attribute__((address_space(3)))
#define GAS __attribute__((address_space(1)))
typedef unsigned short u16;
typedef unsigned int u32;
typedef unsigned long long u64;
typedef short bf16x8 __attribute__((ext_vector_type(8)));
typedef short s16x4 __attribute__((ext_vector_type(4)));
typedef float f32x4 __attribute__((ext_vector_type(4)));
typedef float f32x2 __attribute__((ext_vector_type(2)));
typedef unsigned u32x4 __attribute__((ext_vector_type(4)));
typedef unsigned u32x2 __attribute__((ext_vector_type(2)));
typedef __bf16 bf16x2_t __attribute__((ext_vector_type(2)));

constexpr int SL = 2048, DM = 512, NS = 128, FF = 1376;
constexpr int XS = 2080;
constexpr size_t CTL_OFF = 0;
constexpr int FA_W = 64, FH_W = 64 + 256, FY_W = 64 + 512;
constexpr size_t SG_OFF = 8192;
constexpr size_t XT_OFF = SG_OFF + 4 * 2048 * 8;
constexpr size_t H_OFF  = XT_OFF + 2134016;
constexpr size_t YT_OFF = H_OFF + (size_t)32 * 22 * 8192;
constexpr size_t WS_END = YT_OFF + (size_t)512 * 2048 * 2;
constexpr size_t DIAG_OFF = (size_t)128 << 20;
constexpr int G1_STAGE = 40960;
constexpr int L_U = 32768, L_EF = 66048, EF_CH = 53 * 544;
constexpr int L_MISC = 131584;
constexpr int L_PAR = L_MISC, L_ST = L_MISC + 4096, L_CS = L_MISC + 5120, L_KC = L_MISC + 6656, L_KR = L_MISC + 9280, L_ST2 = L_MISC + 18496, L_LN = L_MISC + 19008;
constexpr int LDS_BYTES = 163840;
static_assert(L_EF + 2 * EF_CH <= L_MISC && L_LN + 256 <= LDS_BYTES, "lds map");

constexpr int CODE_KB = 40;
struct Params {
    const u16 *x, *logA, *aim, *cre, *cim, *logdt, *dskip, *wg, *wu, *wd;
    u16* out; char* ws;
};
#define STAMP(i) do {} while (0)
#define SSTAMP(i) do {} while (0)

__device__ __forceinline__ float bf2f(u32 b) { return __uint_as_float(b << 16); }
__device__ __forceinline__ float bflo(u32 v) { return __uint_as_float(v << 16); }
__device__ __forceinline__ float bfhi(u32 v) { return __uint_as_float(v & 0xffff0000u); }
__device__ __forceinline__ u32 pk2(float a, float b) { bf16x2_t v = {(__bf16)a, (__bf16)b}; return __builtin_bit_cast(u32, v); }
__device__ __forceinline__ float rbf(float a) { return __uint_as_float(pk2(a, a) << 16); }
template <int CTRL, int RM> __device__ __forceinline__ float dpp_add(float v) {
    return v + __int_as_float(__builtin_amdgcn_update_dpp(0, __float_as_int(v), CTRL, RM, 0xf, true));
}
__device__ __forceinline__ float wave_sum(float v) {
    v = dpp_add<0x111, 0xf>(v); v = dpp_add<0x112, 0xf>(v); v = dpp_add<0x114, 0xf>(v); v = dpp_add<0x118, 0xf>(v);
    v = dpp_add<0x142, 0xa>(v); v = dpp_add<0x143, 0xc>(v);
    return __int_as_float(__builtin_amdgcn_readlane(__float_as_int(v), 63));
}
__device__ __forceinline__ f32x4 mfma16(bf16x8 a, bf16x8 b, f32x4 c) { return __builtin_amdgcn_mfma_f32_16x16x32_bf16(a, b, c, 0, 0, 0); }
template <int SC1> __device__ __forceinline__ void glds16(const void* sbase, u32 voff, u32 lds) {
    if (SC1) asm volatile("s_mov_b32 m0, %2\n\ts_nop 0\n\tglobal_load_lds_dwordx4 %0, %1 sc1" ::"v"(voff), "s"(sbase), "s"(lds) : "memory");
    else     asm volatile("s_mov_b32 m0, %2\n\ts_nop 0\n\tglobal_load_lds_dwordx4 %0, %1" ::"v"(voff), "s"(sbase), "s"(lds) : "memory");
}
__device__ __forceinline__ u32 ldsaddr(LAS char* p) { return (u32)(size_t)p; }
typedef short v4i16_t __attribute__((ext_vector_type(4)));
__device__ __forceinline__ s16x4 trrd(LAS char* p) { return __builtin_amdgcn_ds_read_tr16_b64_v4i16((LAS v4i16_t*)p); }
__device__ __forceinline__ bf16x8 cat(s16x4 a, s16x4 b) { return __builtin_shufflevector(a, b, 0, 1, 2, 3, 4, 5, 6, 7); }
#define WAITV0() asm volatile("s_waitcnt vmcnt(0)" ::: "memory")
#define WAITL0() __builtin_amdgcn_s_waitcnt(0xC07F)
#define BAR() do { asm volatile("" ::: "memory"); __builtin_amdgcn_s_barrier(); asm volatile("" ::: "memory"); } while (0)
#define LBAR() do { WAITL0(); BAR(); } while (0)
#define SB0() __builtin_amdgcn_sched_barrier(0)
#define SGB(mask, n) __builtin_amdgcn_sched_group_barrier(mask, n, 0)
template <int N> __device__ __forceinline__ void waitv() { asm volatile("s_waitcnt vmcnt(%0)" ::"n"(N) : "memory"); }
#define RLX_AGENT __ATOMIC_RELAXED, __HIP_MEMORY_SCOPE_AGENT
__device__ __forceinline__ u64 ld_u64(const char* p) { return __hip_atomic_load((GAS u64*)p, RLX_AGENT); }
__device__ __forceinline__ void st_u64(char* p, u64 v) { __hip_atomic_store((GAS u64*)p, v, RLX_AGENT); }
__device__ __forceinline__ void st16_sc1(__amdgpu_buffer_rsrc_t rs, u32 off, u32x4 v) { __builtin_amdgcn_raw_buffer_store_b128(v, rs, off, 0, 16); }
__device__ __forceinline__ void st8_sc1(__amdgpu_buffer_rsrc_t rs, u32 off, u32x2 v) { __builtin_amdgcn_raw_buffer_store_b64(v, rs, off, 0, 16); }
constexpr u32 SPIN_CAP = 1u << 16;
__device__ __forceinline__ void poll_flags(const char* f, int n, u64 tag, int lane) {
    for (u32 sp = 0; sp < SPIN_CAP; ++sp) {
        const bool ok = lane >= n || ld_u64(f + lane * 8) == tag;
        if (__all(ok)) break;
        __builtin_amdgcn_s_sleep(2);
    }
    __builtin_amdgcn_fence(__ATOMIC_ACQUIRE, "wavefront");
}
template <int NR> __device__ __forceinline__ void poll_stats(const char* g, u32 tag, LAS f32x2* dst, int lane) {
    u64 v[NR];
    for (u32 sp = 0; sp < SPIN_CAP; ++sp) {
        bool ok = true;
#pragma unroll
        for (int i = 0; i < NR; ++i) { v[i] = ld_u64(g + (lane + 64 * i) * 8); ok &= (u32)(v[i] >> 32) == tag; }
        if (__all(ok)) break;
        __builtin_amdgcn_s_sleep(2);
    }
#pragma unroll
    for (int i = 0; i < NR; ++i) dst[lane + 64 * i] = f32x2{bflo((u32)v[i]), 1.0f / bfhi((u32)v[i])};
}

struct G1Frag { bf16x8 x[2], f1[3], f2[3]; };
__device__ __forceinline__ void g1_read(G1Frag& f, LAS char* xb, LAS char* w1a, LAS char* w1b, LAS char* w2a, LAS char* w2b) {
    f.x[0] = *(LAS bf16x8*)(xb); f.x[1] = *(LAS bf16x8*)(xb + 2048);
    f.f1[0] = cat(trrd(w1a), trrd(w1a + 768)); f.f2[0] = cat(trrd(w2a), trrd(w2a + 768));
    f.f1[1] = cat(trrd(w1b), trrd(w1b + 768)); f.f2[1] = cat(trrd(w2b), trrd(w2b + 768));
    f.f1[2] = cat(trrd(w1a + 64), trrd(w1a + 64 + 768)); f.f2[2] = cat(trrd(w2a + 64), trrd(w2a + 64 + 768));
}
template <bool CS> __device__ __forceinline__ void g1_mma(const G1Frag& f, f32x4 (&a1)[3][2], f32x4 (&a2)[3][2], f32x4 (&cs)[3], bf16x8 ones) {
#pragma unroll
    for (int j = 0; j < 3; ++j) {
#pragma unroll
        for (int m = 0; m < 2; ++m) { a1[j][m] = mfma16(f.f1[j], f.x[m], a1[j][m]); a2[j][m] = mfma16(f.f2[j], f.x[m], a2[j][m]); }
        if (CS) cs[j] = mfma16(f.f1[j], ones, cs[j]);
    }
}
template <int NM> __device__ __forceinline__ void sched_g1() {
#pragma unroll
    for (int i = 0; i < 12; ++i) { SGB(0x8, 1); SGB(0x100, 1); if (i < 2) SGB(0x100, 1); }
    if (NM > 12) SGB(0x8, NM - 12);
}
struct cpx { float re, im; };
__device__ __forceinline__ cpx cmul(cpx a, cpx b) { return cpx{fmaf(a.re, b.re, -a.im * b.im), fmaf(a.re, b.im, a.im * b.re)}; }
__device__ __forceinline__ cpx cexpp(float arl, float air, float pw) {
    const float mag = __builtin_amdgcn_exp2f(arl * pw), th = __builtin_amdgcn_fractf(air * pw);
    return cpx{mag * __builtin_amdgcn_cosf(th), mag * __builtin_amdgcn_sinf(th)};
}
template <int TG> __device__ __forceinline__ void conv_rho(LAS char* ua, LAS char* ka, f32x4 (&acc)[4]) {
    constexpr int T[4] = {TG ? 1 : 0, TG ? 2 : 3, TG ? 5 : 4, TG ? 6 : 7};
    constexpr int NF = T[3] + 1;
    bf16x8 A[NF], B[NF];
#pragma unroll
    for (int s = 0; s < NF; ++s) {
        const s16x4 lo = *(LAS s16x4*)(ua + s * 512), hi = *(LAS s16x4*)(ua + s * 512 + 8);
        A[s] = cat(lo, hi);
        B[s] = *(LAS bf16x8*)(ka + (7 - s) * 512);
    }
#pragma unroll
    for (int k = 0; k < 4; ++k)
#pragma unroll
        for (int s = 0; s <= T[k]; ++s) acc[k] = mfma16(A[s], B[T[k] - s], acc[k]);
}
struct G2Frag { bf16x8 h[2], w[2]; };
__device__ __forceinline__ void g2_read(G2Frag& f, LAS char* hb, LAS char* w0, LAS char* w1) {
    f.h[0] = *(LAS bf16x8*)(hb); f.h[1] = *(LAS bf16x8*)(hb + 2048);
    f.w[0] = cat(trrd(w0), trrd(w0 + 512)); f.w[1] = cat(trrd(w1), trrd(w1 + 512));
}
__device__ __forceinline__ void g2_mma(const G2Frag& f, f32x4 (&acc)[2][2]) {
#pragma unroll
    for (int n = 0; n < 2; ++n)
#pragma unroll
        for (int m = 0; m < 2; ++m) acc[n][m] = mfma16(f.w[n], f.h[m], acc[n][m]);
}
__device__ __forceinline__ int g2_slot(int s) { const int q = s & 3; return q == 0 ? 0 : q == 1 ? L_EF : q == 2 ? L_EF + 32768 : L_U; }

__device__ __forceinline__ void math_waves(const Params& p, LAS char* lds, const int w) {
    const int tid = threadIdx.x, lane = tid & 63, wid = __builtin_amdgcn_readfirstlane(tid >> 6);
    const int g = lane >> 4, fr = lane & 15, qq = fr >> 2, pp = lane & 3;
    const __amdgpu_buffer_rsrc_t wsr = __builtin_amdgcn_make_buffer_rsrc(p.ws, 0, (int)WS_END, 0x00020000);
    STAMP(0);
    {
        const int rb = w & 63, cq = w >> 6;
        const u64 ep = ld_u64(p.ws + CTL_OFF);
        u32x4 v[4];
#pragma unroll
        for (int i = 0; i < 4; ++i) v[i] = *(const GAS u32x4*)(p.x + (32 * rb + 4 * wid + i) * DM + lane * 8);
        const int ch = 128 * cq + (tid >> 2), q = tid & 3;
        u16 c[8];
#pragma unroll
        for (int i = 0; i < 8; ++i) c[i] = *(const GAS u16*)(p.x + (32 * rb + 8 * q + i) * DM + ch);
        LAS f32x2* st = (LAS f32x2*)(lds + L_LN);
        u32 sv = 0;
#pragma unroll
        for (int i = 0; i < 4; ++i) {
            float f[8];
#pragma unroll
            for (int j = 0; j < 4; ++j) { f[2 * j] = bflo(v[i][j]); f[2 * j + 1] = bfhi(v[i][j]); }
            float s = ((f[0] + f[1]) + (f[2] + f[3])) + ((f[4] + f[5]) + (f[6] + f[7]));
            const float mean = wave_sum(s) * (1.0f / 512.0f);
            float ss = 0.f;
#pragma unroll
            for (int j = 0; j < 8; ++j) { const float d = f[j] - mean; ss = fmaf(d, d, ss); }
            const float var = wave_sum(ss) * (1.0f / 512.0f);
            const float mb = rbf(mean), vb = rbf(var), sd = rbf(sqrtf(rbf(vb + 1e-5f)));
            if (lane == i) { st[4 * wid + i] = f32x2{mb, 1.0f / sd}; sv = (__float_as_uint(mb) >> 16) | (__float_as_uint(sd) & 0xffff0000u); }
        }
        if (lane < 4) st_u64(p.ws + SG_OFF + (cq * 2048 + 32 * rb + 4 * wid + lane) * 8, ((u64)(u32)(ep + 1) << 32) | sv);
        LBAR();
        float o[8];
#pragma unroll
        for (int i = 0; i < 8; ++i) { const f32x2 s = st[8 * q + i]; o[i] = rbf(bf2f(c[i]) - s.x) * s.y; }
        st16_sc1(wsr, (u32)(XT_OFF + ((size_t)ch * XS + 16 + 32 * rb + 8 * q) * 2), u32x4{pk2(o[0], o[1]), pk2(o[2], o[3]), pk2(o[4], o[5]), pk2(o[6], o[7])});
    }
    STAMP(1);
    const int xcd = w & 7, idx = w >> 3, pn = 8 * (xcd & 1) + (idx & 7), cgp = 4 * (xcd >> 1) + (idx >> 3);
    const int f0 = cgp < 11 ? 96 * cgp : 1056 + 64 * (cgp - 11), nch = cgp < 11 ? 12 : 8;
    {
        const int wm = wid & 3, wn = wid >> 2, msel = wm >> 1, ksel = wm & 1;
        f32x4 a1[3][2], a2[3][2], cs[3];
#pragma unroll
        for (int j = 0; j < 3; ++j) { cs[j] = f32x4{0, 0, 0, 0}; for (int m = 0; m < 2; ++m) { a1[j][m] = f32x4{0, 0, 0, 0}; a2[j][m] = f32x4{0, 0, 0, 0}; } }
        int xo[2], wa[2][2], wb[2][2];
#pragma unroll
        for (int o = 0; o < 2; ++o) {
            const int row = 32 * wm + fr, ks = o ^ ksel; xo[o] = row * 128 + (((4 * ks + g) ^ (row & 7)) << 4);
#pragma unroll
            for (int im = 0; im < 2; ++im) {
                const int base = 16384 + ((im ^ msel) * 12288) + (ks * 32 + 8 * g + qq) * 192 + pp * 8;
                wa[o][im] = base + (((3 * wn) ^ (g & 1)) * 32);
                wb[o][im] = base + (((3 * wn + 1) ^ (g & 1)) * 32);
            }
        }
        const bf16x8 ones = {0x3f80, 0x3f80, 0x3f80, 0x3f80, 0x3f80, 0x3f80, 0x3f80, 0x3f80};
        BAR();
        STAMP(2);
        G1Frag A, B;
        g1_read(A, lds + xo[0], lds + wa[0][0], lds + wb[0][0], lds + wa[0][1], lds + wb[0][1]);
        int so = 0, slot = 0;
#pragma unroll 1
        for (int s = 0; s < 8; ++s) {
            LAS char* sb = lds + so;
            g1_read(B, sb + xo[1], sb + wa[1][0], sb + wb[1][0], sb + wa[1][1], sb + wb[1][1]);
            g1_mma<true>(A, a1, a2, cs, ones);
            sched_g1<15>();
            SB0();
            slot = slot == 2 ? 0 : slot + 1;
            so = slot * G1_STAGE;
            WAITL0();
            if (s < 7) { if (s == 3) WAITV0(); BAR(); }
            SB0();
            sb = lds + so;
            g1_read(A, sb + xo[0], sb + wa[0][0], sb + wb[0][0], sb + wa[0][1], sb + wb[0][1]);
            g1_mma<false>(B, a1, a2, cs, ones);
            sched_g1<12>();
            SB0();
        }
        STAMP(3);
        LAS float* cst = (LAS float*)(lds + L_CS);
        if (fr == 0) {
#pragma unroll
            for (int j = 0; j < 3; ++j) *(LAS f32x4*)(cst + (ksel * 2 + msel) * 96 + 16 * (3 * wn + j) + 4 * g) = cs[j];
        }
        LBAR();
        f32x2 st[2];
#pragma unroll
        for (int m = 0; m < 2; ++m) st[m] = *(LAS f32x2*)(lds + L_ST + (32 * wm + 16 * m + fr) * 8);
        const int tok = 128 * pn + 32 * wm + 16 * (g & 1) + fr, rt = tok >> 6, rr = tok & 63;
#pragma unroll
        for (int j = 0; j < 3; ++j) {
            const int t = 3 * wn + j;
            const f32x4 cg = *(LAS f32x4*)(cst + 16 * t + 4 * g) + *(LAS f32x4*)(cst + 192 + 16 * t + 4 * g);
            const f32x4 cu = *(LAS f32x4*)(cst + 96 + 16 * t + 4 * g) + *(LAS f32x4*)(cst + 288 + 16 * t + 4 * g);
            u32 hp[2][2];
#pragma unroll
            for (int m = 0; m < 2; ++m) {
                float h[4];
#pragma unroll
                for (int r = 0; r < 4; ++r) {
                    const float ga = msel ? a2[j][m][r] : a1[j][m][r], ua = msel ? a1[j][m][r] : a2[j][m][r];
                    const float gv = rbf((ga - st[m].x * cg[r]) * st[m].y), uv = rbf((ua - st[m].x * cu[r]) * st[m].y);
                    const float z = gv * fmaf(gv * gv, -0.10294324f, -2.3022082f);
                    const float ge = rbf(gv * __builtin_amdgcn_rcpf(1.0f + __builtin_amdgcn_exp2f(z)));
                    h[r] = ge * uv;
                }
                hp[m][0] = pk2(h[0], h[1]); hp[m][1] = pk2(h[2], h[3]);
            }
            auto s0 = __builtin_amdgcn_permlane16_swap(hp[0][0], hp[1][0], false, false);
            auto s1 = __builtin_amdgcn_permlane16_swap(hp[0][1], hp[1][1], false, false);
            const int chunk = 2 * t + (g >> 1);
            if (chunk < nch) {
                const int f = f0 + 8 * chunk, blk = f >> 6, cc = (f >> 3) & 7;
                st16_sc1(wsr, (u32)(H_OFF + ((size_t)(rt * 22 + blk) * 64 + rr) * 128 + ((cc ^ (rr & 7)) << 4)), u32x4{s0[0], s1[0], s0[1], s1[1]});
            }
        }
    }
    STAMP(4);
    {
        const int chn = tid >> 8, n = tid & 127, half = (tid >> 7) & 1;
        const f32x4 pr = *(LAS f32x4*)(lds + L_PAR + (chn * 128 + n) * 16);
        const float arl = pr[0], air = pr[1]; const cpx c2 = {pr[2], pr[3]};
        LAS char* eb = lds + L_EF + chn * EF_CH + n * 4; LAS char* fb = eb + 21 * 544;
        auto we = [&](int r, cpx v) { *(LAS u32*)(eb + r * 544) = pk2(v.re, -v.im); };
        auto wf = [&](int r, cpx v) { *(LAS u32*)(fb + r * 544) = pk2(v.re, v.im); };
        const cpx w8 = cexpp(arl, air, 8.f), z256 = cexpp(arl, air, 256.f);
        if (half == 0) {
            cpx f = {1.f, 0.f}; wf(0, f);
#pragma unroll
            for (int b = 1; b < 16; ++b) { f = cmul(f, w8); wf(b, f); }
            const cpx z1 = cexpp(arl, air, 1.f); cpx e = c2; we(0, e);
#pragma unroll
            for (int a = 1; a < 8; ++a) { e = cmul(e, z1); we(a, e); }
            const cpx z2 = cmul(z1, z1); e = cmul(c2, z256); we(8, e); e = cmul(e, z2); we(9, e); e = cmul(e, z2); we(10, e); e = cmul(e, z2); we(11, e);
        } else {
            cpx f = cexpp(arl, air, 128.f); wf(16, f);
#pragma unroll
            for (int b = 17; b < 32; ++b) { f = cmul(f, w8); wf(b, f); }
            const cpx z4 = cexpp(arl, air, 4.f), z512 = cexpp(arl, air, 512.f), z1024 = cexpp(arl, air, 1024.f);
            cpx e = cmul(c2, z512); we(12, e); we(13, cmul(e, z4)); e = cmul(e, z256); we(14, e); we(15, cmul(e, z4));
            e = cmul(c2, z1024); we(16, e); e = cmul(e, z256); we(17, e); e = cmul(e, z256); we(18, e); e = cmul(e, z256); we(19, e); e = cmul(e, z256); we(20, e);
        }
    }
    LBAR();
    STAMP(5);
    {
        const int chn = wid >> 2, rt = (wid >> 1) & 1, ct = wid & 1;
        const int er = min(16 * rt + fr, 20);
        LAS char* ea = lds + L_EF + chn * EF_CH + er * 544 + g * 16;
        LAS char* fa = lds + L_EF + chn * EF_CH + (21 + 16 * ct + fr) * 544 + g * 16;
        f32x4 a = {0, 0, 0, 0};
#pragma unroll
        for (int s = 0; s < 8; ++s) a = mfma16(*(LAS bf16x8*)(ea + s * 64), *(LAS bf16x8*)(fa + s * 64), a);
        LAS u16* kc = (LAS u16*)(lds + L_KC + chn * 1296);
        const int beta = 16 * ct + fr;
#pragma unroll
        for (int r = 0; r < 4; ++r) {
            const int al = 16 * rt + 4 * g + r; int ci;
            if (al < 8) ci = al + 8 * beta;
            else if (al < 12) ci = 256 + (al - 8) + 4 * beta;
            else if (al < 16) ci = 384 + 64 * ((al - 12) >> 1) + ((al - 12) & 1) + 2 * beta;
            else if (al < 20) ci = 512 + 32 * (al - 16) + beta;
            else ci = (al == 20 && beta == 0) ? 640 : -1;
            if (ci >= 0) kc[ci] = (u16)pk2(a[r], a[r]);
        }
    }
    LBAR();
    STAMP(6);
    {
        const int chn = tid >> 8, i = tid & 255, m = 255 - i;
        const LAS u16* kc = (const LAS u16*)(lds + L_KC + chn * 1296);
        const int e = m >= 128 ? 3 : m >= 64 ? 2 : m >= 32 ? 1 : 0, hb = ((1 << e) >> 1) - (e ? 1 : 0);
        u32 v[8];
#pragma unroll
        for (int k = 0; k < 8; ++k) { const int j = 8 * m + 7 - k; const int q = (j + hb + (e ? ((j >> e) & 1) : 0)) >> e; v[k] = kc[q + 128 * e]; }
        *(LAS u32x4*)(lds + L_KR + chn * 4608 + i * 16) = u32x4{v[0] | (v[1] << 16), v[2] | (v[3] << 16), v[4] | (v[5] << 16), v[6] | (v[7] << 16)};
        if (i < 32) *(LAS u32x4*)(lds + L_KR + chn * 4608 + 4096 + i * 16) = u32x4{0, 0, 0, 0};
    }
    WAITV0();
    LBAR();
    STAMP(7);
    {
        const int chn = wid & 1, tg = (wid >> 1) & 1, rh = wid >> 2;
        f32x4 acc[4] = {{0, 0, 0, 0}, {0, 0, 0, 0}, {0, 0, 0, 0}, {0, 0, 0, 0}};
        LAS char* ub = lds + L_U + (chn * 4 + (fr & 3)) * 4160 + ((fr >> 2) * 4 + 8 * g) * 2 + rh * 256;
        LAS char* kb = lds + L_KR + chn * 4608 + (240 - 16 * fr + 8 * g) * 2 + rh * 256;
#pragma unroll 1
        for (int i = 0; i < 4; ++i) {
            if (tg == 0) conv_rho<0>(ub + i * 64, kb + i * 64, acc); else conv_rho<1>(ub + i * 64, kb + i * 64, acc);
            if (i == 1) BAR();
        }
        STAMP(8);
        LBAR();
        LAS f32x4* ex = (LAS f32x4*)(lds + L_U) + (wid & 3) * 256;
        if (rh == 0) { ex[lane] = acc[0]; ex[64 + lane] = acc[1]; ex[128 + lane] = acc[2]; ex[192 + lane] = acc[3]; }
        LBAR();
        if (rh == 1) {
            const u32 yb = (u32)(YT_OFF + (size_t)(2 * w + chn) * 4096) + fr * 32 + g * 8;
#pragma unroll
            for (int k = 0; k < 4; ++k) {
                const f32x4 r = acc[k] + ex[64 * k + lane];
                const int t = tg ? (k == 0 ? 1 : k == 1 ? 2 : k == 2 ? 5 : 6) : (k == 0 ? 0 : k == 1 ? 3 : k == 2 ? 4 : 7);
                st8_sc1(wsr, yb + t * 512, u32x2{pk2(r[0], r[1]), pk2(r[2], r[3])});
            }
        }
    }
    STAMP(9);
    {
        const int rt = 4 * xcd + (idx & 3), ct = idx >> 2;
        const int kg = wid >> 2, wm = wid & 1, wn = (wid >> 1) & 1;
        f32x4 acc[2][2] = {{{0, 0, 0, 0}, {0, 0, 0, 0}}, {{0, 0, 0, 0}, {0, 0, 0, 0}}};
        const int row = 32 * wm + fr;
        const int ho = row * 128 + (((4 * kg + g) ^ (row & 7)) << 4);
        const int fk = ((qq >> 1) & 1) | ((g & 1) << 1);
        const int wo0 = 16384 + (32 * kg + 8 * g + qq) * 128 + (((2 * wn) ^ fk) * 32) + pp * 8;
        const int wo1 = 16384 + (32 * kg + 8 * g + qq) * 128 + (((2 * wn + 1) ^ fk) * 32) + pp * 8;
        LBAR();
        STAMP(10);
        G2Frag A, B;
        g2_read(A, lds + ho, lds + wo0, lds + wo1);
#pragma unroll 1
        for (int s = 0; s < 11; ++s) {
            LAS char* sb = lds + g2_slot(s);
            g2_read(B, sb + 8192 + ho, sb + 8192 + wo0, sb + 8192 + wo1);
            g2_mma(A, acc);
            SGB(0x8, 1); SGB(0x100, 2); SGB(0x8, 1); SGB(0x100, 2); SGB(0x8, 1); SGB(0x100, 2); SGB(0x8, 1);
            SB0();
            WAITL0();
            if (s < 10) { if (s == 2) WAITV0(); BAR(); }
            SB0();
            sb = lds + g2_slot(s + 1);
            g2_read(A, sb + ho, sb + wo0, sb + wo1);
            if (s < 10 || kg == 0) g2_mma(B, acc);
            SB0();
        }
        STAMP(11);
        LAS f32x4* ex = (LAS f32x4*)(lds + L_EF) + wid * 128;
        WAITL0();
        BAR();
        ex[lane] = kg ? acc[0][0] : acc[0][1]; ex[64 + lane] = kg ? acc[1][0] : acc[1][1];
        LBAR();
        const LAS f32x4* ey = (const LAS f32x4*)(lds + L_EF) + (wid ^ 4) * 128;
        f32x4 r[2]; r[0] = (kg ? acc[0][1] : acc[0][0]) + ey[lane]; r[1] = (kg ? acc[1][1] : acc[1][0]) + ey[64 + lane];
        const int tl = 32 * wm + 16 * kg + fr;
        const f32x2 st = *(LAS f32x2*)(lds + L_ST2 + tl * 8);
#pragma unroll
        for (int n = 0; n < 2; ++n) {
            const int dl = 32 * wn + 16 * n + 4 * g, d = 64 * ct + dl;
            const u32x2 xv = *(LAS u32x2*)(lds + L_U + tl * 128 + (((dl >> 3) ^ (tl & 7)) << 4) + (dl & 4) * 2), dv = *(const GAS u32x2*)(p.dskip + d);
            float o[4];
#pragma unroll
            for (int j = 0; j < 4; ++j) {
                const float xx = (j & 1) ? bfhi(xv[j >> 1]) : bflo(xv[j >> 1]), ds = (j & 1) ? bfhi(dv[j >> 1]) : bflo(dv[j >> 1]);
                const float y = bf2f(*(LAS u16*)(lds + L_U + 8192 + (dl + j) * 128 + tl * 2));
                const float xn = rbf(rbf(xx - st.x) * st.y);
                const float a = rbf(y + rbf(xn * ds));
                o[j] = rbf(xx + a) + rbf(r[n][j]);
            }
            *(GAS u32x2*)(p.out + (64 * rt + tl) * DM + d) = u32x2{pk2(o[0], o[1]), pk2(o[2], o[3])};
        }
    }
    STAMP(12);
}

__device__ __forceinline__ void dma_waves(const Params& p, LAS char* lds, const int w, const int si) {
    const int lane = threadIdx.x & 63;
    const int xcd = w & 7, idx = w >> 3, pn = 8 * (xcd & 1) + (idx & 7), cgp = 4 * (xcd >> 1) + (idx >> 3);
    const int f0 = cgp < 11 ? 96 * cgp : 1056 + 64 * (cgp - 11), nch = cgp < 11 ? 12 : 8;
    const u32 lb = ldsaddr(lds);
    {
        const u32 vx = (lane >> 3) * 1024 + 16 * ((lane & 7) ^ (lane >> 3));
        u32 vw[12];
#pragma unroll
        for (int i = 0; i < 12; ++i) { const int e = 64 * i + lane, k = e / 12; int c = (e - 12 * k) ^ (((k >> 3) & 1) << 1); if (c >= nch) c -= 8; vw[i] = k * (FF * 2) + 16 * c; }
        const char* xs = (const char*)(p.x + 128 * pn * DM); const char* gs = (const char*)(p.wg + f0); const char* us = (const char*)(p.wu + f0);
        auto issue = [&](int s, int slot) {
            const u32 dst = lb + slot * G1_STAGE;
            if (si == 0) {
#pragma unroll
                for (int i = 0; i < 16; ++i) glds16<0>(xs + s * 128 + i * 8192, vx, dst + i * 1024);
#pragma unroll
                for (int i = 0; i < 4; ++i) glds16<0>(gs + s * (64 * FF * 2), vw[i], dst + 16384 + i * 1024);
            } else {
#pragma unroll
                for (int i = 4; i < 12; ++i) glds16<0>(gs + s * (64 * FF * 2), vw[i], dst + 16384 + i * 1024);
#pragma unroll
                for (int i = 0; i < 12; ++i) glds16<0>(us + s * (64 * FF * 2), vw[i], dst + 28672 + i * 1024);
            }
        };
        issue(0, 0); issue(1, 1);
        BAR();
        issue(2, 2);
        waitv<40>();
        BAR();
        int slot = 0;
#pragma unroll 1
        for (int s = 0; s < 7; ++s) {
            if (s < 6) waitv<20>(); else waitv<0>();
            BAR();
            if (s < 5) issue(s + 3, slot);
            slot = slot == 2 ? 0 : slot + 1;
        }
    }
    BAR();
    const int rt = 4 * xcd + (idx & 3), ct = idx >> 2;
    const u32 vl = lane * 16;
    {
        const char* g = p.ws + XT_OFF + ((size_t)(2 * w + si) * XS + 1) * 2;
#pragma unroll
        for (int cc = 0; cc < 4; ++cc) {
            const u32 dst = lb + L_U + (si * 4 + cc) * 4160;
#pragma unroll
            for (int i = 0; i < 4; ++i) glds16<1>(g + cc * 2 + i * 1024, vl, dst + i * 1024);
            if (lane < 2) glds16<1>(g + cc * 2 + 4096, vl, dst + 4096);
        }
    }
    u32 vw2; { const int k = lane >> 3, cp = lane & 7, f = (k >> 1) & 1; vw2 = k * (DM * 2) + 16 * ((((cp >> 1) ^ f) << 1) | (cp & 1)); }
    const char* wds = (const char*)(p.wd + 64 * ct); const char* hs = p.ws + H_OFF + (size_t)rt * 22 * 8192;
    auto issue2 = [&](int s) {
        const u32 dst = lb + g2_slot(s);
        if (si == 0) {
#pragma unroll
            for (int i = 0; i < 16; ++i) { const int ss = (s == 10 && i >= 12) ? 9 : s;
                glds16<0>(wds + (size_t)(ss * 128 + 8 * i) * (DM * 2), (i & 1) ? (vw2 ^ 64u) : vw2, dst + 16384 + i * 1024); }
        } else {
#pragma unroll
            for (int i = 0; i < 16; ++i) glds16<1>(hs + s * 16384 + i * 1024, vl, dst + i * 1024);
        }
    };
    if (si == 0) issue2(0);
    BAR();
    BAR();
    if (si == 0) { issue2(1); issue2(2); waitv<48>(); } else WAITV0();
    {
        if (lane < 15) {
#pragma unroll
            for (int cc = 0; cc < 4; ++cc) if (lane < 15 - cc) *(LAS u16*)(lds + L_U + (si * 4 + cc) * 4160 + lane * 2) = 0;
        }
    }
    LBAR();
    BAR();
    if (si == 1) { issue2(0); issue2(1); issue2(2); }
    BAR();
    BAR();
    waitv<32>();
    BAR();
    issue2(3);
#pragma unroll 1
    for (int s = 0; s < 10; ++s) {
        if (s < 8) waitv<32>(); else if (s == 8) waitv<24>(); else waitv<8>();
        BAR();
        if (s < 7) issue2(s + 4);
        if (s == 7) {
            if (si == 0) {
                const char* xs = (const char*)(p.x + (64 * rt) * DM + 64 * ct); const u32 vx = (lane >> 3) * 1024 + 16 * ((lane & 7) ^ (lane >> 3));
#pragma unroll
                for (int i = 0; i < 8; ++i) glds16<0>(xs + i * 8192, vx, lb + L_U + i * 1024);
            } else {
                const char* ys = p.ws + YT_OFF + ((size_t)(64 * ct) * SL + 64 * rt) * 2; const u32 vy = (lane >> 3) * 4096 + 16 * (lane & 7);
#pragma unroll
                for (int i = 0; i < 8; ++i) glds16<1>(ys + i * 32768, vy, lb + L_U + 8192 + i * 1024);
            }
        }
    }
    WAITV0();
    BAR();
    BAR();
}
__device__ __forceinline__ void io_wave(const Params& p, LAS char* lds, const int w) {
    const int lane = threadIdx.x & 63;
    const int xcd = w & 7, idx = w >> 3, pn = 8 * (xcd & 1) + (idx & 7), cgp = 4 * (xcd >> 1) + (idx >> 3);
    const int rt = 4 * xcd + (idx & 3), ct = idx >> 2;
    char* ctl = p.ws + CTL_OFF;
    const u64 tag = ld_u64(ctl) + 1;
    BAR();
    BAR();
#pragma unroll 1
    for (int s = 0; s < 7; ++s) {
        if (s == 5) poll_stats<2>(p.ws + SG_OFF + ((w & 3) * 2048 + 128 * pn) * 8, (u32)tag, (LAS f32x2*)(lds + L_ST), lane);
        if (s == 6) poll_flags(ctl + (FA_W + 64 * (w >> 6)) * 8, 64, tag, lane);
        LBAR();
        if (s == 3 && lane == 0) st_u64(ctl + (FA_W + w) * 8, tag);
    }
    BAR();
    BAR();
    BAR();
    BAR();
    if (lane == 0) st_u64(ctl + (FH_W + pn * 16 + cgp) * 8, tag);
    poll_flags(ctl + (FH_W + (rt >> 1) * 16) * 8, 16, tag, lane);
    BAR();
    BAR();
    BAR();
    BAR();
#pragma unroll 1
    for (int s = 0; s < 10; ++s) {
        if (s == 7) {
            poll_flags(ctl + (FY_W + 32 * ct) * 8, 32, tag, lane);
            poll_stats<1>(p.ws + SG_OFF + ((w & 3) * 2048 + 64 * rt) * 8, (u32)tag, (LAS f32x2*)(lds + L_ST2), lane);
        }
        LBAR();
        if (s == 2 && lane == 0) st_u64(ctl + (FY_W + w) * 8, tag);
        if (s == 8 && w == 0) {
#pragma unroll
            for (int i = 0; i < 4; ++i) poll_flags(ctl + (FA_W + 64 * i) * 8, 64, tag, lane);
            if (lane == 0) st_u64(ctl, tag);
        }
    }
    BAR();
    BAR();
}
__device__ __forceinline__ void par_wave(const Params& p, LAS char* lds, const int w) {
    const int lane = threadIdx.x & 63;
    f32x4 pr[4];
#pragma unroll
    for (int i = 0; i < 4; ++i) {
        const int chn = i >> 1, n = (i & 1) * 64 + lane, d = 2 * w + chn;
        const float dt = rbf(__expf(bf2f(p.logdt[d])));
        const float ar = -rbf(__expf(bf2f(p.logA[d * NS + n]))) * dt, ai = bf2f(p.aim[d * NS + n]) * dt;
        pr[i] = f32x4{ar * 1.44269504f, ai * 0.15915494f, 2.0f * bf2f(p.cre[d * NS + n]) * dt, 2.0f * bf2f(p.cim[d * NS + n]) * dt};
    }
    BAR();
    BAR();
#pragma unroll
    for (int i = 0; i < 4; ++i) *(LAS f32x4*)(lds + L_PAR + (i * 64 + lane) * 16) = pr[i];
#pragma unroll 1
    for (int s = 0; s < 7; ++s) LBAR();
#pragma unroll 1
    for (int s = 0; s < 20; ++s) BAR();
}

__global__ __launch_bounds__(768) void k_fused(Params p) {
    extern __shared__ __attribute__((aligned(16))) char smem[];
    LAS char* lds = (LAS char*)smem;
    const int wid = __builtin_amdgcn_readfirstlane(threadIdx.x >> 6), w = blockIdx.x;
    if (wid == 11) {
        const char* pc = (const char*)__builtin_amdgcn_s_getpc();
        const u32 dst = ldsaddr(lds) + L_LN + 256, vo = (threadIdx.x & 63) * 128;
#pragma unroll
        for (int i = 0; i < CODE_KB / 8; ++i)
            asm volatile("s_mov_b32 m0, %2\n\ts_nop 0\n\tglobal_load_lds_dword %0, %1" ::"v"(vo), "s"(pc + i * 8192), "s"(dst) : "memory");
    }
    if (wid < 8) math_waves(p, lds, w);
    else if (wid < 10) dma_waves(p, lds, w, wid - 8);
    else if (wid == 10) io_wave(p, lds, w);
    else par_wave(p, lds, w);
}

extern "C" void kernel_launch(void* const* d_in, const int* in_sizes, int n_in, void* d_out, int out_size, void* d_ws, size_t ws_size, hipStream_t stream) {
    Params p;
    p.x = (const u16*)d_in[0]; p.logA = (const u16*)d_in[1]; p.aim = (const u16*)d_in[2]; p.cre = (const u16*)d_in[3]; p.cim = (const u16*)d_in[4];
    p.logdt = (const u16*)d_in[5]; p.dskip = (const u16*)d_in[6]; p.wg = (const u16*)d_in[8]; p.wu = (const u16*)d_in[9]; p.wd = (const u16*)d_in[10];
    p.out = (u16*)d_out; p.ws = (char*)d_ws;
    (void)hipFuncSetAttribute((const void*)k_fused, hipFuncAttributeMaxDynamicSharedMemorySize, LDS_BYTES);
    k_fused<<<256, 768, LDS_BYTES, stream>>>(p);
}
```

```cpp
#include <hip/hip_runtime.h>
#include <hip/hip_bf16.h>

#define LAS __attribute__((address_space(3)))
#define GAS __attribute__((address_space(1)))
typedef unsigned short u16;
typedef unsigned int u32;
typedef unsigned long long u64;
typedef short bf16x8 __attribute__((ext_vector_type(8)));
typedef short s16x4 __attribute__((ext_vector_type(4)));
typedef float f32x4 __attribute__((ext_vector_type(4)));
typedef float f32x2 __attribute__((ext_vector_type(2)));
typedef unsigned u32x4 __attribute__((ext_vector_type(4)));
typedef unsigned u32x2 __attribute__((ext_vector_type(2)));
typedef __bf16 bf16x2_t __attribute__((ext_vector_type(2)));

constexpr int SL = 2048, DM = 512, NS = 128, FF = 1376;
constexpr int XS = 2080;
constexpr size_t CTL_OFF = 0;
constexpr int FA_W = 64, FH_W = 64 + 256, FY_W = 64 + 512;
constexpr size_t SG_OFF = 8192;
constexpr size_t XT_OFF = SG_OFF + 4 * 2048 * 8;
constexpr size_t H_OFF  = XT_OFF + 2134016;
constexpr size_t YT_OFF = H_OFF + (size_t)32 * 22 * 8192;
constexpr size_t WS_END = YT_OFF + (size_t)512 * 2048 * 2;
constexpr size_t DIAG_OFF = (size_t)128 << 20;
constexpr int G1_STAGE = 40960;
constexpr int L_U = 32768, L_EF = 66048, EF_CH = 53 * 544;
constexpr int L_MISC = 131584;
constexpr int L_CS = L_MISC, L_KC = L_MISC + 1536, L_KR = L_MISC + 4160, L_LN = L_MISC + 13376, L_TAG = L_LN + 256;
constexpr int LDS_BYTES = 163840;
static_assert(L_EF + 2 * EF_CH <= L_MISC && L_TAG + 16 <= LDS_BYTES, "lds map");

struct Params {
    const u16 *x, *logA, *aim, *cre, *cim, *logdt, *dskip, *wg, *wu, *wd;
    u16* out; char* ws;
};
#define STAMP(i) do {} while (0)
#define SSTAMP(i) do {} while (0)

__device__ __forceinline__ float bf2f(u32 b) { return __uint_as_float(b << 16); }
__device__ __forceinline__ float bflo(u32 v) { return __uint_as_float(v << 16); }
__device__ __forceinline__ float bfhi(u32 v) { return __uint_as_float(v & 0xffff0000u); }
__device__ __forceinline__ u32 pk2(float a, float b) { bf16x2_t v = {(__bf16)a, (__bf16)b}; return __builtin_bit_cast(u32, v); }
__device__ __forceinline__ float rbf(float a) { return __uint_as_float(pk2(a, a) << 16); }
template <int CTRL, int RM> __device__ __forceinline__ float dpp_add(float v) {
    return v + __int_as_float(__builtin_amdgcn_update_dpp(0, __float_as_int(v), CTRL, RM, 0xf, true));
}
__device__ __forceinline__ float wave_sum(float v) {
    v = dpp_add<0x111, 0xf>(v); v = dpp_add<0x112, 0xf>(v); v = dpp_add<0x114, 0xf>(v); v = dpp_add<0x118, 0xf>(v);
    v = dpp_add<0x142, 0xa>(v); v = dpp_add<0x143, 0xc>(v);
    return __int_as_float(__builtin_amdgcn_readlane(__float_as_int(v), 63));
}
__device__ __forceinline__ f32x4 mfma16(bf16x8 a, bf16x8 b, f32x4 c) { return __builtin_amdgcn_mfma_f32_16x16x32_bf16(a, b, c, 0, 0, 0); }
template <int SC1> __device__ __forceinline__ void glds16(const void* sbase, u32 voff, u32 lds) {
    if (SC1) asm volatile("s_mov_b32 m0, %2\n\ts_nop 0\n\tglobal_load_lds_dwordx4 %0, %1 sc1" ::"v"(voff), "s"(sbase), "s"(lds) : "memory");
    else     asm volatile("s_mov_b32 m0, %2\n\ts_nop 0\n\tglobal_load_lds_dwordx4 %0, %1" ::"v"(voff), "s"(sbase), "s"(lds) : "memory");
}
__device__ __forceinline__ u32 ldsaddr(LAS char* p) { return (u32)(size_t)p; }
typedef short v4i16_t __attribute__((ext_vector_type(4)));
__device__ __forceinline__ s16x4 trrd(LAS char* p) { return __builtin_amdgcn_ds_read_tr16_b64_v4i16((LAS v4i16_t*)p); }
__device__ __forceinline__ bf16x8 cat(s16x4 a, s16x4 b) { return __builtin_shufflevector(a, b, 0, 1, 2, 3, 4, 5, 6, 7); }
#define WAITV0() asm volatile("s_waitcnt vmcnt(0)" ::: "memory")
#define WAITL0() __builtin_amdgcn_s_waitcnt(0xC07F)
#define BAR() do { asm volatile("" ::: "memory"); __builtin_amdgcn_s_barrier(); asm volatile("" ::: "memory"); } while (0)
#define LBAR() do { WAITL0(); BAR(); } while (0)
#define SB0() __builtin_amdgcn_sched_barrier(0)
#define SGB(mask, n) __builtin_amdgcn_sched_group_barrier(mask, n, 0)
template <int N> __device__ __forceinline__ void waitv() { asm volatile("s_waitcnt vmcnt(%0)" ::"n"(N) : "memory"); }
#define RLX_AGENT __ATOMIC_RELAXED, __HIP_MEMORY_SCOPE_AGENT
__device__ __forceinline__ u64 ld_u64(const char* p) { return __hip_atomic_load((GAS u64*)p, RLX_AGENT); }
__device__ __forceinline__ void st_u64(char* p, u64 v) { __hip_atomic_store((GAS u64*)p, v, RLX_AGENT); }
__device__ __forceinline__ void st16_sc1(__amdgpu_buffer_rsrc_t rs, u32 off, u32x4 v) { __builtin_amdgcn_raw_buffer_store_b128(v, rs, off, 0, 16); }
__device__ __forceinline__ void st8_sc1(__amdgpu_buffer_rsrc_t rs, u32 off, u32x2 v) { __builtin_amdgcn_raw_buffer_store_b64(v, rs, off, 0, 16); }
constexpr u32 SPIN_CAP = 1u << 16;
__device__ __forceinline__ void poll_flags(const char* f, int n, u64 tag, int lane) {
#pragma unroll 1
    for (u32 sp = 0; sp < SPIN_CAP; ++sp) {
        const bool ok = lane >= n || ld_u64(f + lane * 8) == tag;
        if (__all(ok)) break;
        __builtin_amdgcn_s_sleep(2);
    }
    __builtin_amdgcn_fence(__ATOMIC_ACQUIRE, "wavefront");
}
__device__ __forceinline__ u32 settle_granule(const char* g, u64 v, u32 tag) {
#pragma unroll 1
    for (u32 sp = 0; sp < SPIN_CAP && !__all((u32)(v >> 32) == tag); ++sp) { __builtin_amdgcn_s_sleep(2); v = ld_u64(g); }
    return (u32)v;
}
__device__ __forceinline__ float dot2(u32 a, u32 b, float c) { return __builtin_amdgcn_fdot2_f32_bf16(__builtin_bit_cast(bf16x2_t, a), __builtin_bit_cast(bf16x2_t, b), c, false); }
__device__ __forceinline__ float swap32_add(float a, float b) { auto r = __builtin_amdgcn_permlane32_swap(__float_as_uint(a), __float_as_uint(b), false, false); return __uint_as_float(r[0]) + __uint_as_float(r[1]); }
__device__ __forceinline__ float swap16_add(float a, float b) { auto r = __builtin_amdgcn_permlane16_swap(__float_as_uint(a), __float_as_uint(b), false, false); return __uint_as_float(r[0]) + __uint_as_float(r[1]); }
__device__ __forceinline__ float row16_sum(float v) {
    v = dpp_add<0x128, 0xf>(v); v = dpp_add<0x124, 0xf>(v); v = dpp_add<0x122, 0xf>(v); return dpp_add<0x121, 0xf>(v);
}
__device__ __forceinline__ f32x2 pkfma(f32x2 a, f32x2 b, f32x2 c) { return __builtin_elementwise_fma(a, b, c); }
__device__ __forceinline__ f32x2 cmul(f32x2 a, f32x2 b, f32x2 bs) { return pkfma(f32x2{a.y, a.y}, bs, f32x2{a.x, a.x} * b); }
__device__ __forceinline__ f32x2 rot90(f32x2 b) { return f32x2{-b.y, b.x}; }
__device__ __forceinline__ f32x2 cexpp(float arl, float air, float pw) {
    const float mag = __builtin_amdgcn_exp2f(arl * pw), th = __builtin_amdgcn_fractf(air * pw);
    return f32x2{mag * __builtin_amdgcn_cosf(th), mag * __builtin_amdgcn_sinf(th)};
}

struct G1Frag { bf16x8 x[2], f1[3], f2[3]; };
__device__ __forceinline__ void g1_read(G1Frag& f, LAS char* xb, LAS char* w1a, LAS char* w1b, LAS char* w2a, LAS char* w2b) {
    f.x[0] = *(LAS bf16x8*)(xb); f.x[1] = *(LAS bf16x8*)(xb + 2048);
    f.f1[0] = cat(trrd(w1a), trrd(w1a + 768)); f.f2[0] = cat(trrd(w2a), trrd(w2a + 768));
    f.f1[1] = cat(trrd(w1b), trrd(w1b + 768)); f.f2[1] = cat(trrd(w2b), trrd(w2b + 768));
    f.f1[2] = cat(trrd(w1a + 64), trrd(w1a + 64 + 768)); f.f2[2] = cat(trrd(w2a + 64), trrd(w2a + 64 + 768));
}
__device__ __forceinline__ void g1_mma(const G1Frag& f, f32x4 (&a1)[3][2], f32x4 (&a2)[3][2]) {
#pragma unroll
    for (int j = 0; j < 3; ++j)
#pragma unroll
        for (int m = 0; m < 2; ++m) { a1[j][m] = mfma16(f.f1[j], f.x[m], a1[j][m]); a2[j][m] = mfma16(f.f2[j], f.x[m], a2[j][m]); }
}
__device__ __forceinline__ void g1_colsum(const G1Frag& f, int sel, f32x4 (&cs)[3], bf16x8 ones) {
    asm volatile("s_cmp_lg_u32 %10, 0\n\ts_cbranch_scc1 .Lcs_u%=\n\t"
                 "v_mfma_f32_16x16x32_bf16 %0, %3, %9, %0\n\tv_mfma_f32_16x16x32_bf16 %1, %4, %9, %1\n\tv_mfma_f32_16x16x32_bf16 %2, %5, %9, %2\n\t"
                 "s_branch .Lcs_e%=\n.Lcs_u%=:\n\t"
                 "v_mfma_f32_16x16x32_bf16 %0, %6, %9, %0\n\tv_mfma_f32_16x16x32_bf16 %1, %7, %9, %1\n\tv_mfma_f32_16x16x32_bf16 %2, %8, %9, %2\n"
                 ".Lcs_e%=:"
                 : "+v"(cs[0]), "+v"(cs[1]), "+v"(cs[2])
                 : "v"(f.f1[0]), "v"(f.f1[1]), "v"(f.f1[2]), "v"(f.f2[0]), "v"(f.f2[1]), "v"(f.f2[2]), "v"(ones), "s"(sel) : "scc");
}
__device__ __forceinline__ void sched_g1() {
#pragma unroll
    for (int i = 0; i < 7; ++i) { SGB(0x8, 1); SGB(0x100, 2); }
    SGB(0x8, 5);
}
template <int TG> __device__ __forceinline__ void conv_rho(LAS char* ua, LAS char* ka, f32x4 (&acc)[4]) {
    constexpr int T[4] = {TG ? 1 : 0, TG ? 2 : 3, TG ? 5 : 4, TG ? 6 : 7};
    constexpr int NF = T[3] + 1;
    bf16x8 A[NF], B[NF];
#pragma unroll
    for (int s = 0; s < NF; ++s) {
        const s16x4 lo = *(volatile LAS s16x4*)(ua + s * 512), hi = *(volatile LAS s16x4*)(ua + s * 512 + 8);
        A[s] = cat(lo, hi);
        B[s] = *(LAS bf16x8*)(ka + (7 - s) * 512);
    }
#pragma unroll
    for (int k = 0; k < 4; ++k)
#pragma unroll
        for (int s = 0; s <= T[k]; ++s) acc[k] = mfma16(A[s], B[T[k] - s], acc[k]);
}
struct G2Frag { bf16x8 h[2][2], w[2][2]; };
__device__ __forceinline__ void g2_read(G2Frag& f, LAS char* hb, LAS char* w0, LAS char* w1) {
#pragma unroll
    for (int i = 0; i < 2; ++i) {
        f.h[i][0] = *(LAS bf16x8*)(hb + i * 8192); f.h[i][1] = *(LAS bf16x8*)(hb + i * 8192 + 2048);
        f.w[i][0] = cat(trrd(w0 + i * 8192), trrd(w0 + i * 8192 + 512)); f.w[i][1] = cat(trrd(w1 + i * 8192), trrd(w1 + i * 8192 + 512));
    }
}
template <int I> __device__ __forceinline__ void g2_mma(const G2Frag& f, f32x4 (&acc)[2][2]) {
#pragma unroll
    for (int n = 0; n < 2; ++n)
#pragma unroll
        for (int m = 0; m < 2; ++m) acc[n][m] = mfma16(f.w[I][n], f.h[I][m], acc[n][m]);
}
__device__ __forceinline__ int g2_slot(int s) { const int q = s & 3; return q == 0 ? 0 : q == 1 ? L_EF : q == 2 ? L_EF + 32768 : L_U; }

__device__ __forceinline__ void math_waves(const Params& p, LAS char* lds, const int w) {
    const int tid = threadIdx.x, lane = tid & 63, wid = __builtin_amdgcn_readfirstlane(tid >> 6);
    const int g = lane >> 4, fr = lane & 15, qq = fr >> 2, pp = lane & 3;
    const __amdgpu_buffer_rsrc_t wsr = __builtin_amdgcn_make_buffer_rsrc(p.ws, 0, (int)WS_END, 0x00020000);
    char* const ctl = p.ws + CTL_OFF;
    STAMP(0);
    u64 tag; f32x4 par;
    {
        const int rb = w & 63, cq = w >> 6;
        const u64 ep = ld_u64(ctl);
        u32x4 v[4];
#pragma unroll
        for (int i = 0; i < 4; ++i) v[i] = *(const GAS u32x4*)(p.x + (32 * rb + 4 * wid + i) * DM + lane * 8);
        const int ch = 128 * cq + (tid >> 2), q = tid & 3;
        u32 c[8];
#pragma unroll
        for (int i = 0; i < 8; ++i) c[i] = *(const GAS u16*)(p.x + (32 * rb + 8 * q + i) * DM + ch);
        const int pd = 2 * w + (tid >> 8), pi = pd * NS + (tid & 127);
        const u32 q0 = p.logdt[pd], q1 = p.logA[pi], q2 = p.aim[pi], q3 = p.cre[pi], q4 = p.cim[pi];
        float s1[4], s2[4];
#pragma unroll
        for (int i = 0; i < 4; ++i) {
            s1[i] = dot2(v[i][1], 0x3f803f80u, dot2(v[i][0], 0x3f803f80u, 0.f)) + dot2(v[i][3], 0x3f803f80u, dot2(v[i][2], 0x3f803f80u, 0.f));
            s2[i] = dot2(v[i][1], v[i][1], dot2(v[i][0], v[i][0], 0.f)) + dot2(v[i][3], v[i][3], dot2(v[i][2], v[i][2], 0.f));
        }
        const float mean = row16_sum(swap16_add(swap32_add(s1[0], s1[2]), swap32_add(s1[1], s1[3]))) * (1.0f / 512.0f);
        const float msq  = row16_sum(swap16_add(swap32_add(s2[0], s2[2]), swap32_add(s2[1], s2[3]))) * (1.0f / 512.0f);
        const float mb = rbf(mean), sd = rbf(sqrtf(rbf(rbf(fmaxf(msq - mean * mean, 0.f)) + 1e-5f)));
        tag = __builtin_amdgcn_readfirstlane((u32)ep) | ((u64)__builtin_amdgcn_readfirstlane((u32)(ep >> 32)) << 32);
        tag += 1;
        LAS f32x2* st = (LAS f32x2*)(lds + L_LN);
        if (tid == 0) *(LAS u64*)(lds + L_TAG) = tag;
        if (fr == 0) {
            st[4 * wid + g] = f32x2{mb, __builtin_amdgcn_rcpf(sd)};
            st_u64(p.ws + SG_OFF + (cq * 2048 + 32 * rb + 4 * wid + g) * 8, (tag << 32) | (__float_as_uint(mb) >> 16) | (__float_as_uint(sd) & 0xffff0000u));
        }
        LBAR();
        u32 o[4];
#pragma unroll
        for (int i = 0; i < 4; ++i) {
            const f32x4 s = *(LAS f32x4*)(st + 8 * q + 2 * i);
            const u32 d = pk2(bf2f(c[2 * i]) - s[0], bf2f(c[2 * i + 1]) - s[2]);
            o[i] = pk2(bflo(d) * s[1], bfhi(d) * s[3]);
        }
        st16_sc1(wsr, (u32)(XT_OFF + ((size_t)ch * XS + 16 + 32 * rb + 8 * q) * 2), u32x4{o[0], o[1], o[2], o[3]});
        const float dt = rbf(__expf(bf2f(q0)));
        par = f32x4{-rbf(__expf(bf2f(q1))) * dt * 1.44269504f, bf2f(q2) * dt * 0.15915494f, 2.0f * bf2f(q3) * dt, 2.0f * bf2f(q4) * dt};
    }
    STAMP(1);
    const int xcd = w & 7, idx = w >> 3, pn = 8 * (xcd & 1) + (idx & 7), cgp = 4 * (xcd >> 1) + (idx >> 3);
    const int f0 = cgp < 11 ? 96 * cgp : 1056 + 64 * (cgp - 11), nch = cgp < 11 ? 12 : 8;
    {
        const int wm = wid & 3, wn = wid >> 2, msel = wm >> 1, ksel = wm & 1;
        f32x4 a1[3][2], a2[3][2], cs[3];
#pragma unroll
        for (int j = 0; j < 3; ++j) { cs[j] = f32x4{0, 0, 0, 0}; for (int m = 0; m < 2; ++m) { a1[j][m] = f32x4{0, 0, 0, 0}; a2[j][m] = f32x4{0, 0, 0, 0}; } }
        int xo[2], wa[2][2], wb[2][2];
#pragma unroll
        for (int o = 0; o < 2; ++o) {
            const int row = 32 * wm + fr, ks = o ^ ksel; xo[o] = row * 128 + (((4 * ks + g) ^ (row & 7)) << 4);
#pragma unroll
            for (int im = 0; im < 2; ++im) {
                const int base = 16384 + im * 12288 + (ks * 32 + 8 * g + qq) * 192 + pp * 8;
                wa[o][im] = base + (((3 * wn) ^ (g & 1)) * 32);
                wb[o][im] = base + (((3 * wn + 1) ^ (g & 1)) * 32);
            }
        }
        const bf16x8 ones = {0x3f80, 0x3f80, 0x3f80, 0x3f80, 0x3f80, 0x3f80, 0x3f80, 0x3f80};
        const char* sgp = p.ws + SG_OFF + ((w & 3) * 2048 + 128 * pn + 32 * wm + fr) * 8;
        u64 sg0 = 0, sg1 = 0;
        BAR();
        STAMP(2);
        G1Frag A, B;
        g1_read(A, lds + xo[0], lds + wa[0][0], lds + wb[0][0], lds + wa[0][1], lds + wb[0][1]);
        int so = 0, slot = 0;
#pragma unroll 1
        for (int s = 0; s < 8; ++s) {
            LAS char* sb = lds + so;
            g1_read(B, sb + xo[1], sb + wa[1][0], sb + wb[1][0], sb + wa[1][1], sb + wb[1][1]);
            g1_mma(A, a1, a2);
            sched_g1();
            SB0();
            g1_colsum(A, msel, cs, ones);
            SB0();
            slot = slot == 2 ? 0 : slot + 1;
            so = slot * G1_STAGE;
            WAITL0();
            if (s < 7) {
                if (s == 3) WAITV0();
                BAR();
                if (s == 3 && tid == 0) st_u64(ctl + (FA_W + w) * 8, tag);
                if (s == 5) { sg0 = ld_u64(sgp); sg1 = ld_u64(sgp + 128); }
            }
            SB0();
            sb = lds + so;
            g1_read(A, sb + xo[0], sb + wa[0][0], sb + wb[0][0], sb + wa[0][1], sb + wb[0][1]);
            g1_mma(B, a1, a2);
            sched_g1();
            SB0();
        }
        STAMP(3);
        LAS float* cst = (LAS float*)(lds + L_CS);
        if (fr == 0) {
#pragma unroll
            for (int j = 0; j < 3; ++j) *(LAS f32x4*)(cst + (ksel * 2 + msel) * 96 + 16 * (3 * wn + j) + 4 * g) = cs[j];
        }
        LBAR();
        const u32 sv[2] = {settle_granule(sgp, sg0, (u32)tag), settle_granule(sgp + 128, sg1, (u32)tag)};
        float nm[2], k0[2], k1[2], r2[2];
#pragma unroll
        for (int m = 0; m < 2; ++m) {
            const float rs = __builtin_amdgcn_rcpf(bfhi(sv[m]));
            nm[m] = -bflo(sv[m]); r2[m] = rs * rs;
            k0[m] = -2.3022082f * rs; k1[m] = -0.10294324f * rs * r2[m];
        }
        const int tok = 128 * pn + 32 * wm + 16 * (g & 1) + fr, rt = tok >> 6, rr = tok & 63;
#pragma unroll
        for (int j = 0; j < 3; ++j) {
            const int t = 3 * wn + j;
            const f32x4 cg = *(LAS f32x4*)(cst + 16 * t + 4 * g) + *(LAS f32x4*)(cst + 192 + 16 * t + 4 * g);
            const f32x4 cu = *(LAS f32x4*)(cst + 96 + 16 * t + 4 * g) + *(LAS f32x4*)(cst + 288 + 16 * t + 4 * g);
            u32 hp[2][2];
#pragma unroll
            for (int m = 0; m < 2; ++m)
#pragma unroll
                for (int r = 0; r < 2; ++r) {
                    const f32x2 nm2 = {nm[m], nm[m]};
                    const f32x2 tg = pkfma(nm2, f32x2{cg[2 * r], cg[2 * r + 1]}, f32x2{a1[j][m][2 * r], a1[j][m][2 * r + 1]});
                    const f32x2 tu = pkfma(nm2, f32x2{cu[2 * r], cu[2 * r + 1]}, f32x2{a2[j][m][2 * r], a2[j][m][2 * r + 1]});
                    const f32x2 z = tg * pkfma(tg * tg, f32x2{k1[m], k1[m]}, f32x2{k0[m], k0[m]});
                    const f32x2 d = f32x2{__builtin_amdgcn_exp2f(z.x), __builtin_amdgcn_exp2f(z.y)} + 1.0f;
                    const f32x2 h = (tg * tu) * (f32x2{__builtin_amdgcn_rcpf(d.x), __builtin_amdgcn_rcpf(d.y)} * r2[m]);
                    hp[m][r] = pk2(h.x, h.y);
                }
            auto s0 = __builtin_amdgcn_permlane16_swap(hp[0][0], hp[1][0], false, false);
            auto s1 = __builtin_amdgcn_permlane16_swap(hp[0][1], hp[1][1], false, false);
            const int chunk = 2 * t + (g >> 1);
            if (chunk < nch) {
                const int f = f0 + 8 * chunk, blk = f >> 6, cc = (f >> 3) & 7;
                st16_sc1(wsr, (u32)(H_OFF + ((size_t)(rt * 22 + blk) * 64 + rr) * 128 + ((cc ^ (rr & 7)) << 4)), u32x4{s0[0], s1[0], s0[1], s1[1]});
            }
        }
    }
    STAMP(4);
    {
        const int chn = tid >> 8, n = tid & 127, half = (tid >> 7) & 1;
        const float arl = par[0], air = par[1]; const f32x2 c2 = {par[2], -par[3]};
        LAS char* eb = lds + L_EF + chn * EF_CH + n * 4; LAS char* fb = eb + 21 * 544;
        auto we = [&](int r, f32x2 v) { *(LAS u32*)(eb + r * 544) = pk2(v.x, v.y); };
        auto wf = [&](int r, f32x2 v) { *(LAS u32*)(fb + r * 544) = pk2(v.x, v.y); };
        const f32x2 w8 = cexpp(arl, air, 8.f), w8s = rot90(w8), z256 = cexpp(arl, -air, 256.f), z256s = rot90(z256);
        if (half == 0) {
            f32x2 f = w8; wf(0, f32x2{1.f, 0.f}); wf(1, f);
#pragma unroll
            for (int b = 2; b < 16; ++b) { f = cmul(f, w8, w8s); wf(b, f); }
            const f32x2 z1 = cexpp(arl, -air, 1.f), z1s = rot90(z1); f32x2 e = c2; we(0, e);
#pragma unroll
            for (int a = 1; a < 8; ++a) { e = cmul(e, z1, z1s); we(a, e); }
            const f32x2 z2 = cmul(z1, z1, z1s), z2s = rot90(z2);
            e = cmul(c2, z256, z256s); we(8, e); e = cmul(e, z2, z2s); we(9, e); e = cmul(e, z2, z2s); we(10, e); e = cmul(e, z2, z2s); we(11, e);
        } else {
            f32x2 f = cexpp(arl, air, 128.f); wf(16, f);
#pragma unroll
            for (int b = 17; b < 32; ++b) { f = cmul(f, w8, w8s); wf(b, f); }
            const f32x2 z4 = cexpp(arl, -air, 4.f), z4s = rot90(z4), z512 = cexpp(arl, -air, 512.f), z1024 = cexpp(arl, -air, 1024.f);
            f32x2 e = cmul(c2, z512, rot90(z512)); we(12, e); we(13, cmul(e, z4, z4s)); e = cmul(e, z256, z256s); we(14, e); we(15, cmul(e, z4, z4s));
            e = cmul(c2, z1024, rot90(z1024)); we(16, e); e = cmul(e, z256, z256s); we(17, e); e = cmul(e, z256, z256s); we(18, e); e = cmul(e, z256, z256s); we(19, e); e = cmul(e, z256, z256s); we(20, e);
        }
    }
    STAMP(13);
    LBAR();
    STAMP(5);
    {
        const int chn = wid >> 2, rt = (wid >> 1) & 1, ct = wid & 1;
        const int er = min(16 * rt + fr, 20);
        LAS char* ea = lds + L_EF + chn * EF_CH + er * 544 + g * 16;
        LAS char* fa = lds + L_EF + chn * EF_CH + (21 + 16 * ct + fr) * 544 + g * 16;
        f32x4 a = {0, 0, 0, 0};
#pragma unroll
        for (int s = 0; s < 8; ++s) a = mfma16(*(LAS bf16x8*)(ea + s * 64), *(LAS bf16x8*)(fa + s * 64), a);
        const int beta = 16 * ct + fr;
        int c0, c1, c2;
        if (rt == 0) { c0 = g < 2 ? 4 * g + 8 * beta : g == 2 ? 256 + 4 * beta : 384 + 2 * beta; c1 = 1; c2 = g == 3 ? 62 : 0; }
        else { c0 = g == 0 ? 512 + beta : 640; c1 = 32; c2 = 0; }
        LAS u16* kc = (LAS u16*)(lds + L_KC + chn * 1296);
        const u32 k01 = pk2(a[0], a[1]), k23 = pk2(a[2], a[3]);
        if (rt == 0 || g == 0) { kc[c0] = (u16)k01; kc[c0 + c1] = (u16)(k01 >> 16); kc[c0 + 2 * c1 + c2] = (u16)k23; kc[c0 + 3 * c1 + c2] = (u16)(k23 >> 16); }
        else if (g == 1 && beta == 0) kc[640] = (u16)k01;
    }
    LBAR();
    STAMP(6);
    {
        const int chn = tid >> 8, i = tid & 255, m = 255 - i;
        const LAS u16* kc = (const LAS u16*)(lds + L_KC + chn * 1296);
        const int e = m >= 128 ? 3 : m >= 64 ? 2 : m >= 32 ? 1 : 0, hb = ((1 << e) >> 1) - (e ? 1 : 0), em = e ? 1 : 0;
        u32 v[8];
#pragma unroll
        for (int k = 0; k < 8; ++k) { const int j = 8 * m + 7 - k; const int q = (j + hb + ((j >> e) & em)) >> e; v[k] = kc[q + 128 * e]; }
        *(LAS u32x4*)(lds + L_KR + chn * 4608 + i * 16) = u32x4{v[0] | (v[1] << 16), v[2] | (v[3] << 16), v[4] | (v[5] << 16), v[6] | (v[7] << 16)};
        if (i < 32) *(LAS u32x4*)(lds + L_KR + chn * 4608 + 4096 + i * 16) = u32x4{0, 0, 0, 0};
    }
    WAITV0();
    LBAR();
    if (tid == 0) st_u64(ctl + (FH_W + pn * 16 + cgp) * 8, tag);
    STAMP(7);
    {
        const int chn = wid & 1, tg = (wid >> 1) & 1, rh = wid >> 2;
        f32x4 acc[4] = {{0, 0, 0, 0}, {0, 0, 0, 0}, {0, 0, 0, 0}, {0, 0, 0, 0}};
        LAS char* ub = lds + L_U + (chn * 4 + (fr & 3)) * 4160 + ((fr >> 2) * 4 + 8 * g) * 2 + rh * 256;
        LAS char* kb = lds + L_KR + chn * 4608 + (240 - 16 * fr + 8 * g) * 2 + rh * 256;
        if (tg == 0) {
#pragma unroll 1
            for (int i = 0; i < 4; ++i) conv_rho<0>(ub + i * 64, kb + i * 64, acc);
        } else {
#pragma unroll 1
            for (int i = 0; i < 4; ++i) conv_rho<1>(ub + i * 64, kb + i * 64, acc);
        }
        STAMP(8);
        LBAR();
        LAS f32x4* ex = (LAS f32x4*)(lds + L_U) + (wid & 3) * 256;
        if (rh == 0) { ex[lane] = acc[0]; ex[64 + lane] = acc[1]; ex[128 + lane] = acc[2]; ex[192 + lane] = acc[3]; }
        LBAR();
        if (rh == 1) {
            const u32 yb = (u32)(YT_OFF + (size_t)(2 * w + chn) * 4096) + fr * 32 + g * 8;
#pragma unroll
            for (int k = 0; k < 4; ++k) {
                const f32x4 r = acc[k] + ex[64 * k + lane];
                const int t = tg ? (k == 0 ? 1 : k == 1 ? 2 : k == 2 ? 5 : 6) : (k == 0 ? 0 : k == 1 ? 3 : k == 2 ? 4 : 7);
                st8_sc1(wsr, yb + t * 512, u32x2{pk2(r[0], r[1]), pk2(r[2], r[3])});
            }
        }
    }
    STAMP(9);
    {
        const int rt = 4 * xcd + (idx & 3), ct = idx >> 2;
        const int kg = wid >> 2, wm = wid & 1, wn = (wid >> 1) & 1;
        f32x4 acc[2][2] = {{{0, 0, 0, 0}, {0, 0, 0, 0}}, {{0, 0, 0, 0}, {0, 0, 0, 0}}};
        const int row = 32 * wm + fr;
        const int ho = row * 128 + (((4 * kg + g) ^ (row & 7)) << 4);
        const int fk = ((qq >> 1) & 1) | ((g & 1) << 1);
        const int wo0 = 16384 + (32 * kg + 8 * g + qq) * 128 + (((2 * wn) ^ fk) * 32) + pp * 8;
        const int wo1 = 16384 + (32 * kg + 8 * g + qq) * 128 + (((2 * wn + 1) ^ fk) * 32) + pp * 8;
        const int tok = 64 * rt + 32 * wm + 16 * kg + fr, d0 = 64 * ct + 32 * wn + 4 * g;
        const char* sgp = p.ws + SG_OFF + ((w & 3) * 2048 + tok) * 8;
        const char* fyp = ctl + (FY_W + 32 * ct + (lane & 31)) * 8;
        u64 sg = 0, fy = 0; u32 yv[8] = {0, 0, 0, 0, 0, 0, 0, 0}; u32x2 xv[2] = {{0, 0}, {0, 0}}, dv[2] = {{0, 0}, {0, 0}};
        LBAR();
        STAMP(10);
        G2Frag A, B;
        g2_read(A, lds + ho, lds + wo0, lds + wo1);
        WAITL0();
        auto step = [&](G2Frag& cur, G2Frag& nxt, int s) {
            if (s == 2) WAITV0();
            BAR();
            LAS char* sb = lds + g2_slot(s + 1);
            g2_read(nxt, sb + ho, sb + wo0, sb + wo1);
            g2_mma<0>(cur, acc); g2_mma<1>(cur, acc);
            SGB(0x100, 4); SGB(0x8, 1); SGB(0x100, 4); SGB(0x8, 1); SGB(0x100, 4); SGB(0x8, 6);
            SB0();
            if (s == 2 && tid == 0) st_u64(ctl + (FY_W + w) * 8, tag);
            if (s == 6) { fy = ld_u64(fyp); sg = ld_u64(sgp); }
            if (s == 8) {
#pragma unroll 1
                for (u32 sp = 0; sp < SPIN_CAP && !__all(fy == tag); ++sp) { __builtin_amdgcn_s_sleep(2); fy = ld_u64(fyp); }
                __builtin_amdgcn_fence(__ATOMIC_ACQUIRE, "wavefront");
#pragma unroll
                for (int j = 0; j < 8; ++j) yv[j] = __hip_atomic_load((GAS u32*)(p.ws + YT_OFF + ((size_t)(d0 + 16 * (j >> 2) + (j & 3)) * SL + (tok & ~1)) * 2), RLX_AGENT);
#pragma unroll
                for (int n = 0; n < 2; ++n) { xv[n] = *(const GAS u32x2*)(p.x + tok * DM + d0 + 16 * n); dv[n] = *(const GAS u32x2*)(p.dskip + d0 + 16 * n); }
            }
            WAITL0();
            SB0();
        };
#pragma unroll 1
        for (int s = 0; s < 10; s += 2) { step(A, B, s); step(B, A, s + 1); }
        g2_mma<0>(A, acc);
        if (kg == 0) g2_mma<1>(A, acc);
        STAMP(11);
        LAS f32x4* ex = (LAS f32x4*)(lds + L_EF) + wid * 128;
        ex[lane] = kg ? acc[0][0] : acc[0][1]; ex[64 + lane] = kg ? acc[1][0] : acc[1][1];
        LBAR();
        const LAS f32x4* ey = (const LAS f32x4*)(lds + L_EF) + (wid ^ 4) * 128;
        f32x4 r[2]; r[0] = (kg ? acc[0][1] : acc[0][0]) + ey[lane]; r[1] = (kg ? acc[1][1] : acc[1][0]) + ey[64 + lane];
        const u32 sv = settle_granule(sgp, sg, (u32)tag);
        const float mean = bflo(sv), rs = __builtin_amdgcn_rcpf(bfhi(sv));
#pragma unroll
        for (int n = 0; n < 2; ++n) {
            float o[4];
#pragma unroll
            for (int j = 0; j < 4; ++j) {
                const float xx = (j & 1) ? bfhi(xv[n][j >> 1]) : bflo(xv[n][j >> 1]), ds = (j & 1) ? bfhi(dv[n][j >> 1]) : bflo(dv[n][j >> 1]);
                o[j] = (xx + fmaf((xx - mean) * rs, ds, (tok & 1) ? bfhi(yv[4 * n + j]) : bflo(yv[4 * n + j]))) + r[n][j];
            }
            *(GAS u32x2*)(p.out + tok * DM + d0 + 16 * n) = u32x2{pk2(o[0], o[1]), pk2(o[2], o[3])};
        }
    }
    STAMP(12);
}

__device__ __forceinline__ void service_waves(const Params& p, LAS char* lds, const int w, const int si) {
    const int lane = threadIdx.x & 63;
    const int xcd = w & 7, idx = w >> 3, pn = 8 * (xcd & 1) + (idx & 7), cgp = 4 * (xcd >> 1) + (idx >> 3);
    const int f0 = cgp < 11 ? 96 * cgp : 1056 + 64 * (cgp - 11), nch = cgp < 11 ? 12 : 8;
    const u32 lb = ldsaddr(lds);
    char* const ctl = p.ws + CTL_OFF;
    {
        const char* src[10]; u32 vo[10], str[10];
#pragma unroll
        for (int i = 0; i < 10; ++i) {
            const int q = 10 * si + i;
            if (q < 16) { src[i] = (const char*)(p.x + (128 * pn + 8 * q) * DM); str[i] = 128; vo[i] = (lane >> 3) * 1024 + 16 * ((lane & 7) ^ (lane >> 3)); }
            else { const int m = q >= 28, e = 64 * (q - 16 - 12 * m) + lane, k = e / 12; int c = (e - 12 * k) ^ (((k >> 3) & 1) << 1); if (c >= nch) c -= 8;
                   src[i] = (const char*)((m ? p.wu : p.wg) + f0); str[i] = 64 * FF * 2; vo[i] = k * (FF * 2) + 16 * c; }
        }
        auto issue = [&](u32 dst) {
#pragma unroll
            for (int i = 0; i < 10; ++i) { glds16<0>(src[i], vo[i], dst + i * 1024); src[i] += str[i]; }
        };
        const u32 d0 = lb + si * 10240;
        issue(d0); issue(d0 + G1_STAGE);
        BAR();
        issue(d0 + 2 * G1_STAGE);
        waitv<20>();
        BAR();
        int slot = 0;
#pragma unroll 1
        for (int s = 0; s < 7; ++s) {
            if (s < 6) waitv<10>(); else waitv<0>();
            BAR();
            if (s < 5) issue(d0 + slot * G1_STAGE);
            slot = slot == 2 ? 0 : slot + 1;
        }
    }
    const u64 tag = *(LAS u64*)(lds + L_TAG);
    poll_flags(ctl + (FA_W + 64 * (w >> 6)) * 8, 64, tag, lane);
    if (si == 0) SSTAMP(18);
    BAR();
    if (si == 0) SSTAMP(19);
    const u32 vl = lane * 16;
#pragma unroll
    for (int c = 0; c < 2; ++c) {
        const int pc = 2 * si + c;
        const char* g = p.ws + XT_OFF + ((size_t)(2 * w + (pc >> 2)) * XS + (pc & 3) + 1) * 2;
        const u32 dst = lb + L_U + pc * 4160;
#pragma unroll
        for (int i = 0; i < 4; ++i) glds16<1>(g + i * 1024, vl, dst + i * 1024);
        if (lane < 2) glds16<1>(g + 4096, vl, dst + 4096);
    }
    const int rt = 4 * xcd + (idx & 3), ct = idx >> 2;
    u32 vw2 = vl; const char* src2; u32 off2;
    if (si < 2) { const int k = lane >> 3, cp = lane & 7, f = (k >> 1) & 1; vw2 = k * (DM * 2) + 16 * ((((cp >> 1) ^ f) << 1) | (cp & 1));
                  src2 = (const char*)(p.wd + 64 * ct + (size_t)(64 * si) * DM); off2 = 16384 + si * 8192; }
    else { src2 = p.ws + H_OFF + (size_t)rt * 22 * 8192 + (si - 2) * 8192; off2 = (si - 2) * 8192; }
    auto issue2 = [&](int s) {
        const u32 dst = lb + g2_slot(s) + off2;
        if (si < 2) {
            const bool tail = s == 10 && si == 1;
#pragma unroll
            for (int i = 0; i < 8; ++i) glds16<0>(src2 + (size_t)(((tail && i >= 4) ? 9 : s) * 128 + 8 * i) * (DM * 2), (i & 1) ? (vw2 ^ 64u) : vw2, dst + i * 1024);
        } else {
#pragma unroll
            for (int i = 0; i < 8; ++i) glds16<1>(src2 + s * 16384 + i * 1024, vw2, dst + i * 1024);
        }
    };
    if (si < 2) issue2(0);
    if (si == 0) SSTAMP(16); if (si == 3) SSTAMP(17);
    BAR();
    BAR();
    if (si < 2) { issue2(1); issue2(2); waitv<24>(); } else WAITV0();
#pragma unroll
    for (int c = 0; c < 2; ++c) { const int pc = 2 * si + c; if (lane < 15 - (pc & 3)) *(LAS u16*)(lds + L_U + pc * 4160 + lane * 2) = 0; }
    if (si == 0) SSTAMP(20); if (si == 2) SSTAMP(21);
    LBAR();
    if (si >= 2) {
        poll_flags(ctl + (FH_W + (rt >> 1) * 16) * 8, 16, tag, lane);
        if (si == 2) SSTAMP(22);
#pragma unroll 1
        for (int s = 0; s < 3; ++s) issue2(s);
        if (si == 2) SSTAMP(23);
    }
    BAR();
    BAR();
    waitv<16>();
    if (si == 2) SSTAMP(24); if (si == 0) SSTAMP(25);
    BAR();
    issue2(3);
#pragma unroll 1
    for (int s = 0; s < 10; ++s) {
        if (s < 8) waitv<16>(); else if (s == 8) waitv<8>(); else waitv<0>();
        BAR();
        if (s < 7) issue2(s + 4);
    }
    if (w == 0 && si == 0) {
#pragma unroll 1
        for (int i = 0; i < 4; ++i) poll_flags(ctl + (FA_W + 64 * i) * 8, 64, tag, lane);
        if (lane == 0) st_u64(ctl, tag);
    }
    BAR();
}

__global__ __launch_bounds__(768) void k_fused(Params p) {
    extern __shared__ __attribute__((aligned(16))) char smem[];
    LAS char* lds = (LAS char*)smem;
    const int wid = __builtin_amdgcn_readfirstlane(threadIdx.x >> 6), w = blockIdx.x;
    if (wid < 8) math_waves(p, lds, w);
    else service_waves(p, lds, w, wid - 8);
}

extern "C" void kernel_launch(void* const* d_in, const int* in_sizes, int n_in, void* d_out, int out_size, void* d_ws, size_t ws_size, hipStream_t stream) {
    Params p;
    p.x = (const u16*)d_in[0]; p.logA = (const u16*)d_in[1]; p.aim = (const u16*)d_in[2]; p.cre = (const u16*)d_in[3]; p.cim = (const u16*)d_in[4];
    p.logdt = (const u16*)d_in[5]; p.dskip = (const u16*)d_in[6]; p.wg = (const u16*)d_in[8]; p.wu = (const u16*)d_in[9]; p.wd = (const u16*)d_in[10];
    p.out = (u16*)d_out; p.ws = (char*)d_ws;
    (void)hipFuncSetAttribute((const void*)k_fused, hipFuncAttributeMaxDynamicSharedMemorySize, LDS_BYTES);
    k_fused<<<256, 768, LDS_BYTES, stream>>>(p);
}
```

```cpp
#include <hip/hip_runtime.h>
#include <hip/hip_bf16.h>

#define LAS __attribute__((address_space(3)))
#define GAS __attribute__((address_space(1)))
typedef unsigned short u16;
typedef unsigned int u32;
typedef unsigned long long u64;
typedef short bf16x8 __attribute__((ext_vector_type(8)));
typedef short s16x4 __attribute__((ext_vector_type(4)));
typedef float f32x4 __attribute__((ext_vector_type(4)));
typedef float f32x2 __attribute__((ext_vector_type(2)));
typedef unsigned u32x4 __attribute__((ext_vector_type(4)));
typedef unsigned u32x2 __attribute__((ext_vector_type(2)));
typedef __bf16 bf16x2_t __attribute__((ext_vector_type(2)));

constexpr int SL = 2048, DM = 512, NS = 128, FF = 1376;
constexpr int XP = 32, XS = 2048 + 2 * XP;
constexpr size_t CTL_OFF = 0;
constexpr int FA_W = 64, FH_W = 1024, FY_W = 1024 + 2048;
constexpr size_t SG_OFF = 32768;
constexpr size_t XT_OFF = SG_OFF + 4 * 2048 * 8;
constexpr size_t H_OFF  = XT_OFF + 512 * 2176 * 2;
constexpr size_t YT_OFF = H_OFF + (size_t)32 * 22 * 8192;
constexpr size_t WS_END = YT_OFF + (size_t)512 * 2048 * 2;
constexpr int G1_STAGE = 40960;
constexpr int L_EF = 81920, EF_CH = 53 * 544;
constexpr int L_MISC = L_EF + 2 * EF_CH;
constexpr int L_U = 3 * 32768 + 8192;
constexpr int G2_SLOT = 32768;
constexpr int L_Y = 1 * G2_SLOT;
constexpr int KC_CH = 1600;
constexpr int L_KC = L_U + 8 * 4160, L_KR = L_KC + 2 * KC_CH, L_CS = L_KR + 9216, L_LN = L_CS + 1536, L_TAG = L_LN + 384, L_SYN = L_TAG + 16, L_DUM = L_TAG + 128, L_XL = L_DUM + 256;
constexpr int LDS_BYTES = 163840;
static_assert(L_KC >= L_MISC && L_U >= 2 * G1_STAGE && L_XL + 8192 <= 162880 && L_XL % 16 == 0 && L_U % 16 == 0, "lds map");

struct Params {
    const u16 *x, *logA, *aim, *cre, *cim, *logdt, *dskip, *wg, *wu, *wd;
    u16* out; char* ws;
};

__device__ __forceinline__ float bf2f(u32 b) { return __uint_as_float(b << 16); }
__device__ __forceinline__ float bflo(u32 v) { return __uint_as_float(v << 16); }
__device__ __forceinline__ float bfhi(u32 v) { return __uint_as_float(v & 0xffff0000u); }
__device__ __forceinline__ u32 pk2(float a, float b) { bf16x2_t v = {(__bf16)a, (__bf16)b}; return __builtin_bit_cast(u32, v); }
__device__ __forceinline__ float rbf(float a) { return __uint_as_float(pk2(a, a) << 16); }
template <int CTRL, int RM> __device__ __forceinline__ float dpp_add(float v) {
    return v + __int_as_float(__builtin_amdgcn_update_dpp(0, __float_as_int(v), CTRL, RM, 0xf, true));
}
__device__ __forceinline__ float wave_sum(float v) {
    v = dpp_add<0x111, 0xf>(v); v = dpp_add<0x112, 0xf>(v); v = dpp_add<0x114, 0xf>(v); v = dpp_add<0x118, 0xf>(v);
    v = dpp_add<0x142, 0xa>(v); v = dpp_add<0x143, 0xc>(v);
    return __int_as_float(__builtin_amdgcn_readlane(__float_as_int(v), 63));
}
__device__ __forceinline__ f32x4 mfma16(bf16x8 a, bf16x8 b, f32x4 c) { return __builtin_amdgcn_mfma_f32_16x16x32_bf16(a, b, c, 0, 0, 0); }
enum { SY_RDY = 0, SY_E0 = 1, SY_E1 = 2, SY_U = 3, SY_C = 4, SY_X = 5  , SY_F = 9, SY_G = 10, SY_FA = 11, SY_N = 12 };
__device__ __forceinline__ u32 conv_ex(int pair) { return pair < 2 ? 3 * 32768 + pair * 4096 : L_XL + (pair - 2) * 4096; }
template <int SC1> __device__ __forceinline__ void glds16(const void* sbase, u32 voff, u32 lds) {
    if (SC1) asm volatile("s_mov_b32 m0, %2\n\ts_nop 0\n\tglobal_load_lds_dwordx4 %0, %1 sc1" ::"v"(voff), "s"(sbase), "s"(lds) : "memory");
    else     asm volatile("s_mov_b32 m0, %2\n\ts_nop 0\n\tglobal_load_lds_dwordx4 %0, %1" ::"v"(voff), "s"(sbase), "s"(lds) : "memory");
}
__device__ __forceinline__ void touch64(const char* vaddr, u32 lds) {
    asm volatile("s_mov_b32 m0, %1\n\ts_nop 0\n\tglobal_load_lds_dword %0, off" ::"v"(vaddr), "s"(lds) : "memory");
}
template <int SC1> __device__ __forceinline__ void touch(const void* sbase, u32 voff, u32 lds) {
    if (SC1) asm volatile("s_mov_b32 m0, %2\n\ts_nop 0\n\tglobal_load_lds_dword %0, %1 sc1" ::"v"(voff), "s"(sbase), "s"(lds) : "memory");
    else     asm volatile("s_mov_b32 m0, %2\n\ts_nop 0\n\tglobal_load_lds_dword %0, %1" ::"v"(voff), "s"(sbase), "s"(lds) : "memory");
}
struct Lds { __device__ __forceinline__ LAS char* operator+(u32 a) const { return (LAS char*)(size_t)a; } };
typedef short v4i16_t __attribute__((ext_vector_type(4)));
__device__ __forceinline__ s16x4 trrd(LAS char* p) { return __builtin_amdgcn_ds_read_tr16_b64_v4i16((LAS v4i16_t*)p); }
__device__ __forceinline__ bf16x8 cat(s16x4 a, s16x4 b) { return __builtin_shufflevector(a, b, 0, 1, 2, 3, 4, 5, 6, 7); }
#define WAITV0() asm volatile("s_waitcnt vmcnt(0)" ::: "memory")
#define WAITL0() __builtin_amdgcn_s_waitcnt(0xC07F)
#define BAR() do { asm volatile("" ::: "memory"); __builtin_amdgcn_s_barrier(); asm volatile("" ::: "memory"); } while (0)
#define LBAR() do { WAITL0(); BAR(); } while (0)
#define SB0() __builtin_amdgcn_sched_barrier(0)
#define SGB(mask, n) __builtin_amdgcn_sched_group_barrier(mask, n, 0)
template <int N> __device__ __forceinline__ void waitv() { asm volatile("s_waitcnt vmcnt(%0)" ::"n"(N) : "memory"); }
#define RLX_AGENT __ATOMIC_RELAXED, __HIP_MEMORY_SCOPE_AGENT
__device__ __forceinline__ u64 ld_u64(const char* p) { return __hip_atomic_load((GAS u64*)p, RLX_AGENT); }
__device__ __forceinline__ void st_u64(char* p, u64 v) { __hip_atomic_store((GAS u64*)p, v, RLX_AGENT); }
__device__ __forceinline__ void st16_sc1(__amdgpu_buffer_rsrc_t rs, u32 off, u32x4 v) { __builtin_amdgcn_raw_buffer_store_b128(v, rs, off, 0, 16); }
__device__ __forceinline__ void st8_sc1(__amdgpu_buffer_rsrc_t rs, u32 off, u32x2 v) { __builtin_amdgcn_raw_buffer_store_b64(v, rs, off, 0, 16); }
#define G2_RT(xcd, idx) (16 * ((xcd) & 1) + 4 * ((xcd) >> 1) + ((idx) & 3))
constexpr int CODE_LINES = 219;
constexpr u32 SPIN_CAP = 1u << 16;
constexpr int TOUCH1 = 2;
__device__ __forceinline__ void poll_flags(const char* f, int n, u64 tag, int lane) {
#pragma unroll 1
    for (u32 sp = 0; sp < SPIN_CAP; ++sp) {
        const bool ok = lane >= n || ld_u64(f + lane * 8) == tag;
        if (__all(ok)) break;
        __builtin_amdgcn_s_sleep(2);
    }
    __builtin_amdgcn_fence(__ATOMIC_ACQUIRE, "wavefront");
}
__device__ __forceinline__ u32 settle_granule(const char* g, u64 v, u32 tag) {
#pragma unroll 1
    for (u32 sp = 0; sp < SPIN_CAP && !__all((u32)(v >> 32) == tag); ++sp) { __builtin_amdgcn_s_sleep(2); v = ld_u64(g); }
    return (u32)v;
}
__device__ __forceinline__ void syn_arrive(int word, int) {
    u64 ex;
    asm volatile("s_waitcnt lgkmcnt(0)\n\ts_mov_b64 %0, exec\n\ts_mov_b64 exec, 1\n\tds_add_u32 %1, %2\n\ts_mov_b64 exec, %0" : "=&s"(ex) : "v"(L_SYN + 4 * word), "v"(1u) : "memory");
}
__device__ __forceinline__ void syn_signal(int word) {
    u64 ex;
    asm volatile("s_mov_b64 %0, exec\n\ts_mov_b64 exec, 1\n\tds_add_u32 %1, %2\n\ts_mov_b64 exec, %0" : "=&s"(ex) : "v"(L_SYN + 4 * word), "v"(1u) : "memory");
}
__device__ __forceinline__ void syn_await(int word, u32 n) {
    u32 v, left = 64 * SPIN_CAP;
    asm volatile("1:\n\tds_read_b32 %0, %2\n\ts_waitcnt lgkmcnt(0)\n\tv_cmp_gt_u32 vcc, %3, %0\n\ts_cbranch_vccz 2f\n\ts_sleep 1\n\ts_sub_u32 %1, %1, 1\n\ts_cbranch_scc0 1b\n2:"
                 : "=&v"(v), "+s"(left) : "v"(L_SYN + 4 * word), "s"(n) : "vcc", "scc", "memory");
}
__device__ __forceinline__ float dot2(u32 a, u32 b, float c) { return __builtin_amdgcn_fdot2_f32_bf16(__builtin_bit_cast(bf16x2_t, a), __builtin_bit_cast(bf16x2_t, b), c, false); }
__device__ __forceinline__ float swap32_add(float a, float b) { auto r = __builtin_amdgcn_permlane32_swap(__float_as_uint(a), __float_as_uint(b), false, false); return __uint_as_float(r[0]) + __uint_as_float(r[1]); }
__device__ __forceinline__ float swap16_add(float a, float b) { auto r = __builtin_amdgcn_permlane16_swap(__float_as_uint(a), __float_as_uint(b), false, false); return __uint_as_float(r[0]) + __uint_as_float(r[1]); }
__device__ __forceinline__ float row16_sum(float v) {
    v = dpp_add<0x128, 0xf>(v); v = dpp_add<0x124, 0xf>(v); v = dpp_add<0x122, 0xf>(v); return dpp_add<0x121, 0xf>(v);
}
__device__ __forceinline__ f32x2 pkfma(f32x2 a, f32x2 b, f32x2 c) { return __builtin_elementwise_fma(a, b, c); }
__device__ __forceinline__ f32x2 cmul(f32x2 a, f32x2 b, f32x2 bs) { return pkfma(f32x2{a.y, a.y}, bs, f32x2{a.x, a.x} * b); }
__device__ __forceinline__ f32x2 rot90(f32x2 b) { return f32x2{-b.y, b.x}; }
__device__ __forceinline__ void cmul2(f32x2& r1, f32x2 a1, f32x2 b1, f32x2& r2, f32x2 a2, f32x2 b2) {
    f32x2 t1, t2;
    asm("v_pk_mul_f32 %0, %2, %3 op_sel_hi:[0,1]\n\tv_pk_mul_f32 %1, %4, %5 op_sel_hi:[0,1]\n\t"
        "v_pk_fma_f32 %0, %2, %3, %0 op_sel:[1,1,0] op_sel_hi:[1,0,1] neg_lo:[1,0,0]\n\t"
        "v_pk_fma_f32 %1, %4, %5, %1 op_sel:[1,1,0] op_sel_hi:[1,0,1] neg_lo:[1,0,0]"
        : "=&v"(t1), "=&v"(t2) : "v"(a1), "v"(b1), "v"(a2), "v"(b2));
    r1 = t1; r2 = t2;
}
__device__ __forceinline__ f32x2 cmul1(f32x2 a, f32x2 b) {
    f32x2 t;
    asm("v_pk_mul_f32 %0, %1, %2 op_sel_hi:[0,1]\n\tv_pk_fma_f32 %0, %1, %2, %0 op_sel:[1,1,0] op_sel_hi:[1,0,1] neg_lo:[1,0,0]" : "=&v"(t) : "v"(a), "v"(b));
    return t;
}
__device__ __forceinline__ void dot_rows(const u32x4 (&v)[4], float (&s1)[4], float (&s2)[4]) {
    const u32 ones = 0x3f803f80u;
    asm("v_dot2_f32_bf16 %0, %8, %24, 0\n\tv_dot2_f32_bf16 %1, %12, %24, 0\n\tv_dot2_f32_bf16 %2, %16, %24, 0\n\tv_dot2_f32_bf16 %3, %20, %24, 0\n\t"
        "v_dot2_f32_bf16 %4, %8, %8, 0\n\tv_dot2_f32_bf16 %5, %12, %12, 0\n\tv_dot2_f32_bf16 %6, %16, %16, 0\n\tv_dot2_f32_bf16 %7, %20, %20, 0\n\t"
        "v_dot2_f32_bf16 %0, %9, %24, %0\n\tv_dot2_f32_bf16 %1, %13, %24, %1\n\tv_dot2_f32_bf16 %2, %17, %24, %2\n\tv_dot2_f32_bf16 %3, %21, %24, %3\n\t"
        "v_dot2_f32_bf16 %4, %9, %9, %4\n\tv_dot2_f32_bf16 %5, %13, %13, %5\n\tv_dot2_f32_bf16 %6, %17, %17, %6\n\tv_dot2_f32_bf16 %7, %21, %21, %7\n\t"
        "v_dot2_f32_bf16 %0, %10, %24, %0\n\tv_dot2_f32_bf16 %1, %14, %24, %1\n\tv_dot2_f32_bf16 %2, %18, %24, %2\n\tv_dot2_f32_bf16 %3, %22, %24, %3\n\t"
        "v_dot2_f32_bf16 %4, %10, %10, %4\n\tv_dot2_f32_bf16 %5, %14, %14, %5\n\tv_dot2_f32_bf16 %6, %18, %18, %6\n\tv_dot2_f32_bf16 %7, %22, %22, %7\n\t"
        "v_dot2_f32_bf16 %0, %11, %24, %0\n\tv_dot2_f32_bf16 %1, %15, %24, %1\n\tv_dot2_f32_bf16 %2, %19, %24, %2\n\tv_dot2_f32_bf16 %3, %23, %24, %3\n\t"
        "v_dot2_f32_bf16 %4, %11, %11, %4\n\tv_dot2_f32_bf16 %5, %15, %15, %5\n\tv_dot2_f32_bf16 %6, %19, %19, %6\n\tv_dot2_f32_bf16 %7, %23, %23, %7\n\t"
        "s_nop 2"
        : "=&v"(s1[0]), "=&v"(s1[1]), "=&v"(s1[2]), "=&v"(s1[3]), "=&v"(s2[0]), "=&v"(s2[1]), "=&v"(s2[2]), "=&v"(s2[3])
        : "v"(v[0][0]), "v"(v[0][1]), "v"(v[0][2]), "v"(v[0][3]), "v"(v[1][0]), "v"(v[1][1]), "v"(v[1][2]), "v"(v[1][3]),
          "v"(v[2][0]), "v"(v[2][1]), "v"(v[2][2]), "v"(v[2][3]), "v"(v[3][0]), "v"(v[3][1]), "v"(v[3][2]), "v"(v[3][3]), "s"(ones));
}
template <int OFF> __device__ __forceinline__ void wr_row(u32 base, u32 a) {
    asm volatile("s_mov_b32 m0, %1\n\ts_nop 0\n\tds_write_addtid_b32 %0 offset:%2" ::"v"(a), "s"(base), "n"(OFF) : "memory");
}
template <int OFF1, int OFF2> __device__ __forceinline__ void wr_rows(u32 base, u32 a, u32 b) {
    asm volatile("s_mov_b32 m0, %2\n\ts_nop 0\n\tds_write_addtid_b32 %0 offset:%3\n\tds_write_addtid_b32 %1 offset:%4" ::"v"(a), "v"(b), "s"(base), "n"(OFF1), "n"(OFF2) : "memory");
}
__device__ __forceinline__ u32 ldu16(const u16* base, u32 off) { return *(const GAS u16*)((const GAS char*)base + off); }
#define PIN(x) asm volatile("" : "+v"(x))
__device__ __forceinline__ f32x2 cexpp(float arl, float air, float pw) {
    const float mag = __builtin_amdgcn_exp2f(arl * pw), th = __builtin_amdgcn_fractf(air * pw);
    return f32x2{mag * __builtin_amdgcn_cosf(th), mag * __builtin_amdgcn_sinf(th)};
}

__device__ __forceinline__ void geglu4(u32& o0, u32& o1, f32x4 g, f32x4 u, f32x4 cg, f32x4 cu, float nm, float k0, float k1, float v2) {
    float a0, a1, a2, a3, b0, b1, b2, b3, c0, c1, c2, c3;
    asm("v_fma_f32 %2, %30, %22, %14\n\tv_fma_f32 %3, %30, %23, %15\n\tv_fma_f32 %4, %30, %24, %16\n\tv_fma_f32 %5, %30, %25, %17\n\t"
        "v_mul_f32 %6, %2, %2\n\tv_mul_f32 %7, %3, %3\n\tv_mul_f32 %8, %4, %4\n\tv_mul_f32 %9, %5, %5\n\t"
        "v_fma_f32 %10, %30, %26, %18\n\tv_fma_f32 %11, %30, %27, %19\n\tv_fma_f32 %12, %30, %28, %20\n\tv_fma_f32 %13, %30, %29, %21\n\t"
        "v_fma_f32 %6, %6, %32, %31\n\tv_fma_f32 %7, %7, %32, %31\n\tv_fma_f32 %8, %8, %32, %31\n\tv_fma_f32 %9, %9, %32, %31\n\t"
        "v_mul_f32 %6, %2, %6\n\tv_mul_f32 %7, %3, %7\n\tv_mul_f32 %8, %4, %8\n\tv_mul_f32 %9, %5, %9\n\t"
        "v_exp_f32 %6, %6\n\tv_exp_f32 %7, %7\n\tv_exp_f32 %8, %8\n\tv_exp_f32 %9, %9\n\t"
        "v_mul_f32 %2, %2, %10\n\tv_mul_f32 %3, %3, %11\n\tv_mul_f32 %4, %4, %12\n\tv_mul_f32 %5, %5, %13\n\t"
        "v_fma_f32 %6, %6, %33, %33\n\tv_fma_f32 %7, %7, %33, %33\n\tv_fma_f32 %8, %8, %33, %33\n\tv_fma_f32 %9, %9, %33, %33\n\t"
        "v_rcp_f32 %6, %6\n\tv_rcp_f32 %7, %7\n\tv_rcp_f32 %8, %8\n\tv_rcp_f32 %9, %9\n\t"
        "v_mul_f32 %2, %2, %6\n\tv_mul_f32 %3, %3, %7\n\tv_mul_f32 %4, %4, %8\n\tv_mul_f32 %5, %5, %9\n\t"
        "v_cvt_pk_bf16_f32 %0, %2, %3\n\tv_cvt_pk_bf16_f32 %1, %4, %5\n\ts_nop 1"
        : "=&v"(o0), "=&v"(o1), "=&v"(a0), "=&v"(a1), "=&v"(a2), "=&v"(a3), "=&v"(b0), "=&v"(b1), "=&v"(b2), "=&v"(b3), "=&v"(c0), "=&v"(c1), "=&v"(c2), "=&v"(c3)
        : "v"(g[0]), "v"(g[1]), "v"(g[2]), "v"(g[3]), "v"(u[0]), "v"(u[1]), "v"(u[2]), "v"(u[3]),
          "v"(cg[0]), "v"(cg[1]), "v"(cg[2]), "v"(cg[3]), "v"(cu[0]), "v"(cu[1]), "v"(cu[2]), "v"(cu[3]), "v"(nm), "v"(k0), "v"(k1), "v"(v2));
}
struct G1Frag { bf16x8 x[4], f[3]; };
__device__ __forceinline__ void g1_read(G1Frag& f, LAS char* xa, LAS char* xb, LAS char* w0, LAS char* w2) {
    f.f[0] = cat(trrd(w0), trrd(w0 + 768)); f.x[0] = *(LAS bf16x8*)(xa); f.x[1] = *(LAS bf16x8*)(xa + 2048);
    f.f[1] = cat(trrd(w0 + 12288), trrd(w0 + 12288 + 768)); f.x[2] = *(LAS bf16x8*)(xb); f.x[3] = *(LAS bf16x8*)(xb + 2048);
    f.f[2] = cat(trrd(w2), trrd(w2 + 768));
}
__device__ __forceinline__ void g1_read_ordered(G1Frag& f, LAS char* xa, LAS char* xb, LAS char* w0, LAS char* w2) {
    f.f[0] = cat(trrd(w0), trrd(w0 + 768)); SB0(); f.x[0] = *(LAS bf16x8*)(xa); SB0(); f.x[1] = *(LAS bf16x8*)(xa + 2048); SB0();
    f.f[1] = cat(trrd(w0 + 12288), trrd(w0 + 12288 + 768)); SB0(); f.x[2] = *(LAS bf16x8*)(xb); SB0(); f.x[3] = *(LAS bf16x8*)(xb + 2048); SB0();
    f.f[2] = cat(trrd(w2), trrd(w2 + 768)); SB0();
}
template <int J> __device__ __forceinline__ void g1_mma_j(const G1Frag& f, f32x4 (&a)[3][4]) {
#pragma unroll
    for (int m = 0; m < 4; ++m) a[J][m] = mfma16(f.f[J], f.x[m], a[J][m]);
}
__device__ __forceinline__ void g1_mma(const G1Frag& f, f32x4 (&a)[3][4]) { g1_mma_j<0>(f, a); g1_mma_j<1>(f, a); g1_mma_j<2>(f, a); }
__device__ __forceinline__ void g1_colsum(const G1Frag& f, f32x4 (&cs)[3], bf16x8 ones) {
#pragma unroll
    for (int j = 0; j < 3; ++j) cs[j] = mfma16(f.f[j], ones, cs[j]);
}
template <int TAIL> __device__ __forceinline__ void sched_g1() {
#pragma unroll
    for (int i = 0; i < 10; ++i) { SGB(0x8, 1); SGB(0x100, 1); }
    SGB(0x8, 2 + TAIL);
}
struct ConvFrag { bf16x8 A[8], B[8]; };
__device__ __forceinline__ bf16x8 conv_rd_u(LAS char* a) {
    const s16x4 lo = *(volatile LAS s16x4*)(a), hi = *(volatile LAS s16x4*)(a + 8);
    return cat(lo, hi);
}
template <int TG, int NEXT> __device__ __forceinline__ void conv_step(ConvFrag& f, f32x4 (&acc)[4], LAS char* ua, LAS char* ka) {
    constexpr int T[4] = {TG ? 1 : 0, TG ? 2 : 3, TG ? 5 : 4, TG ? 6 : 7}, NF = TG ? 7 : 8;
    bf16x8 nb[NF];
#pragma unroll
    for (int s = NF - 1; s >= 0; --s) {
#pragma unroll
        for (int k = 3; k >= 0; --k) if (T[k] >= s) acc[k] = mfma16(f.A[s], f.B[T[k] - s], acc[k]);
        SB0();
        if (NEXT) { f.A[s] = conv_rd_u(ua + s * 512); nb[NF - 1 - s] = *(LAS bf16x8*)(ka + (7 - (NF - 1 - s)) * 512); }
        SB0();
    }
    if (NEXT) {
#pragma unroll
        for (int s = 0; s < NF; ++s) f.B[s] = nb[s];
    }
}
template <int TG> __device__ __forceinline__ void conv_steps(ConvFrag& f, LAS char* ub, LAS char* kb, f32x4 (&acc)[4], char* fh, u64 tag, int lane, bool early) {
    asm volatile("" ::: "memory");
    syn_await(SY_U, 4);
#pragma unroll
    for (int s = 7; s >= 0; --s) { f.A[s] = conv_rd_u(ub + s * 512); f.B[7 - s] = *(LAS bf16x8*)(kb + s * 512); }
    SB0();
    if (early) { WAITV0(); if (lane == 0) st_u64(fh, tag); syn_signal(SY_F); }
    SB0();
#pragma unroll
    for (int i = 1; i < 4; ++i) {
        conv_step<TG, 1>(f, acc, ub + 64 * i, kb + 64 * i);
        if (i == 1 && !early) { WAITV0(); if (lane == 0) st_u64(fh, tag); }
    }
    conv_step<TG, 0>(f, acc, ub, kb);
}
struct G2Frag { bf16x8 h[2][2], w[2][2]; };
__device__ __forceinline__ void g2_read(G2Frag& f, LAS char* hb, LAS char* hc, LAS char* w0, LAS char* w1) {
#pragma unroll
    for (int i = 0; i < 2; ++i) {
        f.h[i][0] = *(LAS bf16x8*)(hb + i * 8192); f.h[i][1] = *(LAS bf16x8*)(hc + i * 8192);
        f.w[i][0] = cat(trrd(w0 + i * 8192), trrd(w0 + i * 8192 + 512)); f.w[i][1] = cat(trrd(w1 + i * 8192), trrd(w1 + i * 8192 + 512));
    }
}
template <int I> __device__ __forceinline__ void g2_mma(const G2Frag& f, f32x4 (&acc)[2][2]) {
#pragma unroll
    for (int n = 0; n < 2; ++n)
#pragma unroll
        for (int m = 0; m < 2; ++m) acc[n][m] = mfma16(f.w[I][n], f.h[I][m], acc[n][m]);
}
__device__ __forceinline__ int g2_slot(int s) { return (s >= 10 ? s - 10 : s >= 5 ? s - 5 : s) * G2_SLOT; }
__device__ __forceinline__ int g2_pos(int cgp, int t) { return t < 3 ? 3 * cgp + t : cgp < 11 ? 48 + 3 * cgp + (t - 3) : 81 + (cgp - 11); }
__device__ __forceinline__ int g2_tile(int q) {
    if (q >= 86) return 0;
    if (q >= 81) return 66 + 4 * (q - 81) + 3;
    const int l = q >= 48, r = q - 48 * l, c = (r * 43) >> 7, j = r - 3 * c + 3 * l;
    return (c < 11 ? 6 * c : 66 + 4 * (c - 11)) + j;
}

__device__ __forceinline__ void math_waves(const Params& p, const int w) {
    const Lds lds;
    const int tid = threadIdx.x, lane = tid & 63, wid = __builtin_amdgcn_readfirstlane(tid >> 6);
    const int g = lane >> 4, fr = lane & 15, qq = fr >> 2, pp = lane & 3;
    const __amdgpu_buffer_rsrc_t wsr = __builtin_amdgcn_make_buffer_rsrc(p.ws, 0, (int)WS_END, 0x00020000);
    char* const ctl = p.ws + CTL_OFF;

    const int chn = wid >> 2, hf = (wid >> 1) & 1, n = tid & 127;
    const int rb = w & 63, cq = w >> 6;
    const u32 po = (u32)((2 * w + chn) * NS + n) * 2;
    const u32 q0 = *(const __attribute__((address_space(4))) u32*)(p.logdt + 2 * w);
    const u32 q1 = ldu16(p.logA, po), q2 = ldu16(p.aim, po), q3 = ldu16(p.cre, po), q4 = ldu16(p.cim, po);
    const u64 ep = ld_u64(ctl);
    SB0();
    const u32 tbase = L_EF + chn * EF_CH + hf * 544 + (wid & 1) * 256;
    const int krt = (wid >> 1) & 1, kct = wid & 1;
    u32 kea = L_EF + chn * EF_CH + (16 * krt + fr) * 544 + g * 16, kfa = L_EF + chn * EF_CH + (21 + 16 * kct + fr) * 544 + g * 16;
    PIN(kea); PIN(kfa);
    const int lq = tid & 3;
    u32 xlr = L_XL + 8 * lq * 256 + ((((tid >> 5) & 15) ^ (2 * lq)) << 4) + ((tid >> 2) & 7) * 2, xlw = L_XL + (4 * wid + g) * 256 + ((fr ^ (2 * (wid >> 1))) << 4);
    PIN(xlr); PIN(xlw);
    const int rh1 = wid & 1, odd = (wid >> 1) & 1, wn = wid >> 2, ta = 3 * wn + 2 * odd, tb = 3 * wn + 1;
    u32 kc0, kc1, xa[8], xo[2], xp[2], w0[2], w2[2];
    auto addr_kc = [&]() {
      const int beta = 16 * kct + fr;
      if (krt == 0) { kc0 = g < 2 ? 4 * g + 8 * beta : g == 2 ? 256 + 4 * beta : 384 + 2 * beta; kc1 = kc0 + (g == 3 ? 64 : 2); }
      else { kc0 = g == 0 ? 512 + beta : (g == 1 && beta == 0) ? 640 : 648 + lane; kc1 = g == 0 ? kc0 + 32 : 648 + lane; }
      kc0 = L_KC + chn * KC_CH + kc0 * 2; kc1 = L_KC + chn * KC_CH + kc1 * 2; PIN(kc0); PIN(kc1); };
    auto addr_xa = [&]() {
      const int wq = wid & 3, m = (tid & 255) ^ 255;
      u32 eS = min(3, 4 - wq), loS = wq < 2 ? 0u : 0x02000000u + (wq - 2) * 0x02020000u, hiS = wq < 2 ? 0x02020200u : 0x04040202u + (wq - 2) * 0x04020202u;
      u32 par = wq < 2 ? 2u : 0u, zl = wq == 3 ? 32u : 64u;
      asm("" : "+s"(eS), "+s"(loS), "+s"(hiS), "+s"(par), "+s"(zl));
      const bool z = (u32)lane >= zl;
      const u32 e = z ? 0u : eS, lo = z ? 0x06040200u : loS, hi = z ? 0x0E0C0A08u : hiS | (((u32)m << 1) & par);
      const u32 base = L_KC + chn * KC_CH + (((u32)m << 4) >> e) + (e << 8);
#pragma unroll
      for (int k = 0; k < 8; ++k) { const int r = 7 - k; xa[k] = base + (((r < 4 ? lo : hi) >> (8 * (r & 3))) & 0xffu); PIN(xa[k]); }
    };
    auto addr_g1 = [&]() {
        const int row = 64 * rh1 + 32 * odd + fr, base = 16384 + (rh1 * 32 + 8 * g + qq) * 192 + pp * 8, dk = rh1 ? -6144 : 6144;
        xo[0] = row * 128 + (((4 * rh1 + g) ^ (row & 7)) << 4); xo[1] = xo[0] ^ 64;
        xp[0] = xo[0] ^ 4096; xp[1] = xo[0] ^ (4096 + 64);
        w0[0] = base + ((ta ^ (g & 1)) * 32); w2[0] = base + odd * 12288 + ((tb ^ (g & 1)) * 32);
        w0[1] = w0[0] + dk; w2[1] = w2[0] + dk;
#pragma unroll
        for (int o = 0; o < 2; ++o) { PIN(xo[o]); PIN(xp[o]); PIN(w0[o]); PIN(w2[o]); }
    };
    addr_kc(); addr_xa(); addr_g1();
    SB0();
    u32x4 v[4];
#pragma unroll
    for (int i = 0; i < 4; ++i) v[i] = *(const GAS u32x4*)((const GAS char*)p.x + (u32)((32 * rb + 4 * wid) * (DM * 2) + lane * 16 + i * (DM * 2)));
    const u32x4 vq = *(const GAS u32x4*)((const GAS char*)p.x + (u32)((32 * rb + 4 * wid + g) * (DM * 2) + cq * 256 + fr * 16));
    SB0();
    {
        const float dt = rbf(__expf(chn ? bfhi(q0) : bflo(q0)));
        const float arl = -rbf(__expf(bf2f(q1))) * dt * 1.44269504f, air = -bf2f(q2) * dt * 0.15915494f;
        const f32x2 c2 = {2.0f * bf2f(q3) * dt, -2.0f * bf2f(q4) * dt}, one = {1.f, 0.f};
        auto pe = [](f32x2 a) { return pk2(a.x, a.y); };
        auto pf = [](f32x2 a) { return pk2(a.x, -a.y); };
        const f32x2 z1 = cexpp(arl, air, 1.f), z256 = cexpp(arl, air, 256.f);
        f32x2 z2, z4, z8, z16, z512, gq, kq, lq2, f, e;
        cmul2(z2, z1, z1, z512, z256, z256);
        cmul2(z4, z2, z2, gq, c2, z256);
        cmul2(z8, z4, z4, e, c2, hf ? z1 : one);
        cmul2(z16, z8, z8, kq, gq, z256);
        f = hf ? z8 : one;
#define TW2(rf, re) do { const u32 a_ = pf(f), b_ = pe(e); wr_rows<(21 + (rf)) * 544, (re) * 544>(tbase, a_, b_); } while (0)
        TW2(0, 0);   cmul2(f, f, z16, e, e, z2);
        TW2(2, 2);   cmul2(f, f, z16, e, e, z2);
        TW2(4, 4);   cmul2(f, f, z16, e, e, z2);
        TW2(6, 6);   cmul2(f, f, z16, e, gq, hf ? z2 : one);
        TW2(8, 8);   cmul2(f, f, z16, e, e, z4);
        TW2(10, 10); cmul2(f, f, z16, e, kq, hf ? z4 : one);
        TW2(12, 12); cmul2(f, f, z16, e, e, z256);
        TW2(14, 14); cmul2(f, f, z16, lq2, kq, z512);
        wr_row<(21 + 16) * 544>(tbase, pf(f)); cmul2(f, f, z16, e, lq2, hf ? z256 : one);
        TW2(18, 16); cmul2(f, f, z16, e, e, z512);
        TW2(20, 18); cmul2(f, f, z16, e, e, z512);
        { const u32 a_ = pf(f), b_ = pe(e); wr_row<(21 + 22) * 544>(tbase, a_); if (hf == 0) wr_row<20 * 544>(tbase, b_); } f = cmul1(f, z16);
        wr_row<(21 + 24) * 544>(tbase, pf(f)); f = cmul1(f, z16);
        wr_row<(21 + 26) * 544>(tbase, pf(f)); f = cmul1(f, z16);
        wr_row<(21 + 28) * 544>(tbase, pf(f)); f = cmul1(f, z16);
        wr_row<(21 + 30) * 544>(tbase, pf(f));
#undef TW2
    }
    LAS float* const stm = (LAS float*)(lds + L_LN);
    {
        *(LAS u32x4*)(lds + xlw) = vq;
        float s1[4], s2[4];
        dot_rows(v, s1, s2);
        const float mean = row16_sum(swap16_add(swap32_add(s1[0], s1[2]), swap32_add(s1[1], s1[3]))) * (1.0f / 512.0f);
        const float msq  = row16_sum(swap16_add(swap32_add(s2[0], s2[2]), swap32_add(s2[1], s2[3]))) * (1.0f / 512.0f);
        const float mb = rbf(mean), sd = rbf(__builtin_amdgcn_sqrtf(rbf(rbf(fmaxf(msq - mean * mean, 0.f)) + 1e-5f)));
        if (tid < 4 + SY_N) *(LAS u32*)(lds + L_TAG + 4 * tid) = tid == 0 ? (u32)(ep + 1) : tid == 1 ? (u32)((ep + 1) >> 32) : 0u;
        if (fr == 0) { stm[4 * wid + g] = mb; stm[32 + 4 * wid + g] = __builtin_amdgcn_rcpf(sd); ((LAS u32*)stm)[64 + 4 * wid + g] = (__float_as_uint(mb) >> 16) | (__float_as_uint(sd) & 0xffff0000u); }
    }
    u64 tag;
    LBAR();
    u32 o[4];
    {
        bf16x8 ea[8], fa[8];
#pragma unroll
        for (int s = 0; s < 8; ++s) { ea[s] = *(LAS bf16x8*)(lds + kea + s * 64); fa[s] = *(LAS bf16x8*)(lds + kfa + s * 64); }
        f32x4 sm[2], sr[2];
#pragma unroll
        for (int i = 0; i < 2; ++i) { sm[i] = *(LAS f32x4*)(stm + 8 * lq + 4 * i); sr[i] = *(LAS f32x4*)(stm + 32 + 8 * lq + 4 * i); }
        u32 c[8];
#pragma unroll
        for (int i = 0; i < 8; ++i) c[i] = *(LAS u16*)(lds + xlr + i * 256);
        const u32x2 tg2 = *(LAS u32x2*)(lds + L_TAG);
        SB0();
        SB0();
        tag = (u64)(u32)__builtin_amdgcn_readfirstlane(tg2[0]) | ((u64)(u32)__builtin_amdgcn_readfirstlane(tg2[1]) << 32);
        f32x4 a0 = {0, 0, 0, 0}, a1 = {0, 0, 0, 0};
#pragma unroll
        for (int s = 0; s < 4; ++s) { a0 = mfma16(ea[2 * s], fa[2 * s], a0); a1 = mfma16(ea[2 * s + 1], fa[2 * s + 1], a1); }
#pragma unroll
        for (int i = 0; i < 4; ++i) {
            const f32x4 m = sm[i >> 1], r = sr[i >> 1];
            const u32 d = pk2(bf2f(c[2 * i]) - m[2 * (i & 1)], bf2f(c[2 * i + 1]) - m[2 * (i & 1) + 1]);
            o[i] = pk2(bflo(d) * r[2 * (i & 1)], bfhi(d) * r[2 * (i & 1) + 1]);
        }
        const f32x4 a = a0 + a1;
        const u32 k01 = pk2(a[0], a[1]), k23 = pk2(a[2], a[3]);
        if (krt == 0) { *(LAS u32*)(lds + kc0) = k01; *(LAS u32*)(lds + kc1) = k23; }
        else { *(LAS u16*)(lds + kc0) = (u16)k01; *(LAS u16*)(lds + kc1) = (u16)(k01 >> 16); *(LAS u16*)(lds + kc0 + 128) = (u16)k23; *(LAS u16*)(lds + kc1 + 128) = (u16)(k23 >> 16); }
    }
    LBAR();
    {
        const int i = tid & 255;
        u32 y[8];
#pragma unroll
        for (int k = 0; k < 8; ++k) y[k] = *(LAS u16*)(lds + xa[k]);
        *(LAS u32x4*)(lds + L_KR + chn * 4608 + i * 16) = u32x4{y[0] | (y[1] << 16), y[2] | (y[3] << 16), y[4] | (y[5] << 16), y[6] | (y[7] << 16)};
        if (i < 32) *(LAS u32x4*)(lds + L_KR + chn * 4608 + 4096 + i * 16) = u32x4{0, 0, 0, 0};
    }
    *(LAS u32x4*)(lds + L_XL + tid * 16) = u32x4{o[0], o[1], o[2], o[3]};

    ConvFrag cf; u32 ubo, kbo;
    const int xcd = w & 7, idx = w >> 3, pn = 8 * (xcd & 1) + (idx & 7), cgp = 4 * (xcd >> 1) + (idx >> 3);
    const int wide = (cgp - 11) >> 31;
    const int nch = 8 + (wide & 4);
    {
        f32x4 a[3][4], cs[3];
        const u32 o1 = fr == 0 ? 0x3f803f80u : 0u;
        bf16x8 ones = __builtin_bit_cast(bf16x8, u32x4{o1, o1, o1, o1});
        PIN(ones);
        const char* sgp = p.ws + SG_OFF + ((w & 3) * 2048 + 128 * pn + 64 * rh1 + fr) * 8;
        u64 sg[4] = {0, 0, 0, 0};
        LBAR();

        G1Frag A, B;
        g1_read_ordered(A, lds + xo[0], lds + xp[0], lds + w0[0], lds + w2[0]);
        {
#pragma unroll
            for (int j = 0; j < 3; ++j) { cs[j] = f32x4{0, 0, 0, 0}; for (int m = 0; m < 4; ++m) a[j][m] = f32x4{0, 0, 0, 0}; }
            g1_read(B, lds + xo[1], lds + xp[1], lds + w0[1], lds + w2[1]);
            g1_mma(A, a);
            g1_colsum(A, cs, ones);
            sched_g1<3>();
            SB0();
        }
        int so = G1_STAGE;
#pragma unroll
        for (int s = 0; s < 7; ++s) {
            WAITL0();
            if (s == 1) {
                const u32x4 ov = *(LAS u32x4*)(lds + L_XL + tid * 16);
                st16_sc1(wsr, (u32)(XT_OFF + ((size_t)(128 * cq + (tid >> 2)) * XS + XP + 32 * rb + 8 * (tid & 3)) * 2), ov);
                if (wid == 7 && lane < 32) st_u64(p.ws + SG_OFF + (cq * 2048 + 32 * rb + lane) * 8, (tag << 32) | ((LAS u32*)(lds + L_LN))[64 + lane]);
            }
            if (s == 2) WAITV0();

            BAR();
            if (s == 2 && tid == 0) st_u64(ctl + (FA_W + w) * 8, tag);
            if (s == 5) {
#pragma unroll
                for (int m = 0; m < 4; ++m) sg[m] = ld_u64(sgp + 128 * (m & 1) + 256 * (((m >> 1) ^ odd)));
            }
            SB0();
            LAS char* sb = lds + so;
            g1_read(A, sb + xo[0], sb + xp[0], sb + w0[0], sb + w2[0]);
            g1_mma(B, a);
            sched_g1<0>();
            SB0();
            g1_read(B, sb + xo[1], sb + xp[1], sb + w0[1], sb + w2[1]);
            g1_mma(A, a);
            g1_colsum(A, cs, ones);
            sched_g1<3>();
            SB0();
            so = so == 2 * G1_STAGE ? 0 : so + G1_STAGE;
        }
        LAS float* cst = (LAS float*)(lds + L_CS);
        LAS f32x4* gx = (LAS f32x4*)(lds + (wn ? 3 * 32768 : L_XL));
        g1_mma_j<2>(B, a); g1_mma_j<0>(B, a);
        SB0();
        if (fr == 0) {
            *(LAS f32x4*)(cst + rh1 * 192 + 16 * ta + 4 * g) = cs[0]; *(LAS f32x4*)(cst + rh1 * 192 + 96 + 16 * ta + 4 * g) = cs[1];
            *(LAS f32x4*)(cst + rh1 * 192 + 96 * odd + 16 * tb + 4 * g) = cs[2];
        }
        gx[(wid & 3) * 128 + lane] = a[2][2]; gx[(wid & 3) * 128 + 64 + lane] = a[2][3];
        syn_arrive(SY_E0 + wn, lane);
        SB0();
        g1_mma_j<1>(B, a);
        SB0();
        u32 sv[4];
        if (!__all((u32)(sg[0] >> 32) == (u32)tag && (u32)(sg[1] >> 32) == (u32)tag && (u32)(sg[2] >> 32) == (u32)tag && (u32)(sg[3] >> 32) == (u32)tag)) {
#pragma unroll 1
            for (int m = 0; m < 4; ++m) { const u64 v_ = settle_granule(sgp + 128 * (m & 1) + 256 * (((m >> 1) ^ odd)), (u64)~(u32)tag << 32, (u32)tag); if (m == 0) sg[0] = v_; else if (m == 1) sg[1] = v_; else if (m == 2) sg[2] = v_; else sg[3] = v_; }
        }
#pragma unroll
        for (int m = 0; m < 4; ++m) sv[m] = (u32)sg[m];
        syn_await(SY_E0 + wn, 4);
        f32x4 c1[4], c2[4], rx[2];
#pragma unroll
        for (int i = 0; i < 4; ++i) { const int o = (i & 1) * 96 + 16 * (i < 2 ? ta : tb) + 4 * g; c1[i] = *(LAS f32x4*)(cst + o); c2[i] = *(LAS f32x4*)(cst + 192 + o); }
        rx[0] = gx[((wid & 3) ^ 2) * 128 + lane]; rx[1] = gx[((wid & 3) ^ 2) * 128 + 64 + lane];
        SB0();
        u32 ho[3];
        { const u32 hv = (u32)H_OFF + (16 * (g & 1) + fr) * 32 + (g >> 1) * 16;
          const u32 hb = (u32)(2 * pn + rh1) * 88 * 2048, pb = wn * (((wide & (45 + 3 * cgp)) | (~wide & (67 + cgp))) - 3 * cgp) + 3 * cgp;
          const u32 ha = hb + (pb + ta) * 2048 + (2 * ta < nch ? 0u : 0x70000000u), hc = hb + (pb + tb) * 2048 + (2 * tb < nch ? 0u : 0x70000000u);
          ho[0] = hv + ha + odd * 1024; ho[1] = hv + ha + (1 - odd) * 1024; ho[2] = hv + hc + odd * 1024; }
        ubo = L_U + ((wid & 1) * 4 + (fr & 3)) * 4160 + ((fr >> 2) * 4 + 8 * g) * 2 + (wid >> 2) * 256;
        kbo = L_KR + (wid & 1) * 4608 + (240 - 16 * fr + 8 * g) * 2 + (wid >> 2) * 256;
        float nm[4], k0[4], k1[4], v2[4];
#pragma unroll
        for (int m = 0; m < 4; ++m) {
            const float sd = bfhi(sv[m]), rs = __builtin_amdgcn_rcpf(sd);
            nm[m] = -bflo(sv[m]); v2[m] = sd * sd;
            k0[m] = -2.3022082f * rs; k1[m] = -0.10294324f * rs * rs * rs;
        }
#pragma unroll
        for (int i = 0; i < 4; ++i) c1[i] += c2[i];
        if (wn) syn_arrive(SY_G, lane);
        auto geglu = [&](const f32x4 g0, const f32x4 u0, const f32x4 g1, const f32x4 u1, const f32x4 cg, const f32x4 cu, const int m0, const u32 dst) {
            u32 hp[2][2];
#pragma unroll
            for (int mm = 0; mm < 2; ++mm)
#pragma unroll
                for (int r = 0; r < 2; ++r) {
                    const int m = m0 + mm; const f32x4 ag = mm ? g1 : g0, au = mm ? u1 : u0;
                    const f32x2 nm2 = {nm[m], nm[m]}, v22 = {v2[m], v2[m]};
                    const f32x2 tg = pkfma(nm2, f32x2{cg[2 * r], cg[2 * r + 1]}, f32x2{ag[2 * r], ag[2 * r + 1]});
                    const f32x2 tu = pkfma(nm2, f32x2{cu[2 * r], cu[2 * r + 1]}, f32x2{au[2 * r], au[2 * r + 1]});
                    const f32x2 z = tg * pkfma(tg * tg, f32x2{k1[m], k1[m]}, f32x2{k0[m], k0[m]});
                    const f32x2 d = pkfma(f32x2{__builtin_amdgcn_exp2f(z.x), __builtin_amdgcn_exp2f(z.y)}, v22, v22);
                    const f32x2 h = (tg * tu) * f32x2{__builtin_amdgcn_rcpf(d.x), __builtin_amdgcn_rcpf(d.y)};
                    hp[mm][r] = pk2(h.x, h.y);
                }
            auto s0 = __builtin_amdgcn_permlane16_swap(hp[0][0], hp[1][0], false, false);
            auto s1 = __builtin_amdgcn_permlane16_swap(hp[0][1], hp[1][1], false, false);
            st16_sc1(wsr, dst, u32x4{s0[0], s1[0], s0[1], s1[1]});
        };
        geglu(a[0][0], a[1][0], a[0][1], a[1][1], c1[0], c1[1], 0, ho[0]);
        geglu(a[0][2], a[1][2], a[0][3], a[1][3], c1[0], c1[1], 2, ho[1]);
        SB0();
        auto geglu_plain = [&](const f32x4 g0, const f32x4 u0, const f32x4 g1, const f32x4 u1, const f32x4 cg, const f32x4 cu, const int m0, const u32 dst) {
            u32 h00, h01, h10, h11;
            geglu4(h00, h01, g0, u0, cg, cu, nm[m0], k0[m0], k1[m0], v2[m0]);
            geglu4(h10, h11, g1, u1, cg, cu, nm[m0 + 1], k0[m0 + 1], k1[m0 + 1], v2[m0 + 1]);
            auto s0 = __builtin_amdgcn_permlane16_swap(h00, h10, false, false);
            auto s1 = __builtin_amdgcn_permlane16_swap(h01, h11, false, false);
            st16_sc1(wsr, dst, u32x4{s0[0], s1[0], s0[1], s1[1]});
        };
        if (wn) { if (odd) geglu_plain(rx[0], a[2][0], rx[1], a[2][1], c1[2], c1[3], 0, ho[2]); else geglu_plain(a[2][0], rx[0], a[2][1], rx[1], c1[2], c1[3], 0, ho[2]); }
        else if (odd) geglu(rx[0], a[2][0], rx[1], a[2][1], c1[2], c1[3], 0, ho[2]); else geglu(a[2][0], rx[0], a[2][1], rx[1], c1[2], c1[3], 0, ho[2]);
    }

    {
        const int chn = wid & 1, tg = (wid >> 1) & 1, rh = wid >> 2;
        f32x4 acc[4] = {{0, 0, 0, 0}, {0, 0, 0, 0}, {0, 0, 0, 0}, {0, 0, 0, 0}};
        char* const fh = ctl + (FH_W + (2 * pn + (wid & 1)) * 64 + (wid >> 2) * 32 + cgp * 2 + ((wid >> 1) & 1)) * 8;
        LAS f32x4* ex = (LAS f32x4*)(lds + conv_ex(wid & 3));
        if (tg == 0) conv_steps<0>(cf, lds + ubo, lds + kbo, acc, fh, tag, lane, wid < 4); else conv_steps<1>(cf, lds + ubo, lds + kbo, acc, fh, tag, lane, wid < 4);
        if (rh == 0) { syn_await(SY_F, 4); syn_await(SY_G, 4); }
        if (rh == 0) { ex[lane] = acc[0]; ex[64 + lane] = acc[1]; ex[128 + lane] = acc[2]; ex[192 + lane] = acc[3]; syn_arrive(SY_X + (wid & 3), lane); }
        else syn_await(SY_X + (wid & 3), 1);
        if (rh == 1) {
            const u32 yb = (u32)(YT_OFF + (size_t)(2 * w + chn) * 4096) + fr * 32 + g * 8;
#pragma unroll
            for (int k = 0; k < 4; ++k) {
                const f32x4 r = acc[k] + ex[64 * k + lane];
                const int t = tg ? (k == 0 ? 1 : k == 1 ? 2 : k == 2 ? 5 : 6) : (k == 0 ? 0 : k == 1 ? 3 : k == 2 ? 4 : 7);
                st8_sc1(wsr, yb + t * 512, u32x2{pk2(r[0], r[1]), pk2(r[2], r[3])});
            }
        }
    }
    {
        const int rt = G2_RT(xcd, idx), ct = idx >> 2;
        const int kg = wid >> 2, wm = wid & 1, wn = (wid >> 1) & 1;
        f32x4 acc[2][2] = {{{0, 0, 0, 0}, {0, 0, 0, 0}}, {{0, 0, 0, 0}, {0, 0, 0, 0}}};
        const int row = 32 * wm + fr;
        const int ho = (2 * kg + (g >> 1)) * 2048 + (row + 16 * kg) * 32 + (g & 1) * 16, hq = ho ^ 512;
        const int fk = ((qq >> 1) & 1) | ((g & 1) << 1);
        const int wo0 = 16384 + (32 * kg + 8 * g + qq) * 128 + (((2 * wn) ^ fk) * 32) + pp * 8;
        const int wo1 = 16384 + (32 * kg + 8 * g + qq) * 128 + (((2 * wn + 1) ^ fk) * 32) + pp * 8;
        const int tok = 64 * rt + 32 * wm + 16 * kg + fr;
        const char* sgp = p.ws + SG_OFF + ((w & 3) * 2048 + tok) * 8;
        LBAR();

        G2Frag A, B;
        g2_read(A, lds + ho, lds + hq, lds + wo0, lds + wo1);
        SB0();
        auto step = [&](G2Frag& cur, G2Frag& nxt, int s) {
            BAR();
            LAS char* sb = lds + g2_slot(s + 1);
            g2_read(nxt, sb + ho, sb + hq, sb + wo0, sb + wo1);
            g2_mma<0>(cur, acc); g2_mma<1>(cur, acc);
            SGB(0x100, 2); SGB(0x8, 1); SGB(0x100, 2); SGB(0x8, 1); SGB(0x100, 2); SGB(0x8, 1); SGB(0x100, 2); SGB(0x8, 1); SGB(0x100, 2); SGB(0x8, 1); SGB(0x100, 2); SGB(0x8, 3);
            SB0();
            asm volatile("" : "+v"(acc[0][0]), "+v"(acc[0][1]), "+v"(acc[1][0]), "+v"(acc[1][1]));
        };
        step(A, B, 0);
        if (wid >= 4) { WAITV0(); if (lane == 0) st_u64(ctl + (FY_W + w * 4 + (wid & 1) * 2 + ((wid >> 1) & 1)) * 8, tag); }
        step(B, A, 1);
        step(A, B, 2);
        step(B, A, 3);
        step(A, B, 4);
        step(B, A, 5);
        step(A, B, 6);
        step(B, A, 7);
        u64 sg = ld_u64(sgp);
        const int d8 = 64 * ct + 32 * wn + 8 * g;
        const u32x4 xw = *(const GAS u32x4*)(p.x + tok * DM + d8), dw = *(const GAS u32x4*)(p.dskip + d8);
        f32x4 tx[2];
        auto xpart = [&]() {
            if (!__all((u32)(sg >> 32) == (u32)tag)) sg = settle_granule(sgp, sg, (u32)tag);
            const float mean = bflo((u32)sg), rs = __builtin_amdgcn_rcpf(bfhi((u32)sg));
#pragma unroll
            for (int h = 0; h < 2; ++h)
#pragma unroll
                for (int j = 0; j < 4; ++j) {
                    const float xx = (j & 1) ? bfhi(xw[2 * h + (j >> 1)]) : bflo(xw[2 * h + (j >> 1)]), ds = (j & 1) ? bfhi(dw[2 * h + (j >> 1)]) : bflo(dw[2 * h + (j >> 1)]);
                    tx[h][j] = fmaf((xx - mean) * rs, ds, xx);
                }
            PIN(tx[0]); PIN(tx[1]);
        };
        SB0();
        const int hw = ho - kg * 4096, ww0 = wo0 - kg * 4096, ww1 = wo1 - kg * 4096;
        bf16x8 h9[4], w9[4][2], h10[3], w10[3][2];
        BAR();
        {   LAS char* sb = lds + g2_slot(9);
#pragma unroll
            for (int j = 0; j < 4; ++j) {
                h9[j] = *(LAS bf16x8*)(sb + hw + j * 4096);
                w9[j][0] = cat(trrd(sb + ww0 + j * 4096), trrd(sb + ww0 + j * 4096 + 512)); w9[j][1] = cat(trrd(sb + ww1 + j * 4096), trrd(sb + ww1 + j * 4096 + 512));
            }
            g2_mma<0>(A, acc); g2_mma<1>(A, acc);
            SGB(0x100, 3); SGB(0x8, 1); SGB(0x100, 3); SGB(0x8, 1); SGB(0x100, 3); SGB(0x8, 1); SGB(0x100, 3); SGB(0x8, 1); SGB(0x100, 3); SGB(0x8, 1); SGB(0x100, 3); SGB(0x8, 1); SGB(0x100, 2); SGB(0x8, 2);
            SB0();
            asm volatile("" : "+v"(acc[0][0]), "+v"(acc[0][1]), "+v"(acc[1][0]), "+v"(acc[1][1]));
        }
        LAS f32x4* ex = (LAS f32x4*)(lds + L_Y + 8192) + wid * 128;
        ex[lane] = acc[0][1]; ex[64 + lane] = acc[1][1];
        LBAR();
        s16x4 yq[2];
        const LAS f32x4* ey = (const LAS f32x4*)(lds + L_Y + 8192) + (wid ^ 4) * 128 + (g >> 1) * 64 + (g & 1) * 32 + fr;
        f32x4 e0, e1;
        {   LAS char* sb = lds + g2_slot(10);
#pragma unroll
            for (int j = 0; j < 3; ++j) {
                h10[j] = *(LAS bf16x8*)(sb + hw + j * 4096);
                w10[j][0] = cat(trrd(sb + ww0 + j * 4096), trrd(sb + ww0 + j * 4096 + 512)); w10[j][1] = cat(trrd(sb + ww1 + j * 4096), trrd(sb + ww1 + j * 4096 + 512));
            }
            e0 = ey[0]; e1 = ey[16];
#pragma unroll
            for (int h = 0; h < 2; ++h) yq[h] = trrd(lds + L_Y + (32 * wn + 8 * g + 4 * h + qq) * 128 + (32 * wm + 16 * kg) * 2 + pp * 8);
#pragma unroll
            for (int j = 0; j < 4; ++j) { acc[0][0] = mfma16(w9[j][0], h9[j], acc[0][0]); acc[1][0] = mfma16(w9[j][1], h9[j], acc[1][0]); }
            SGB(0x100, 3); SGB(0x8, 1); SGB(0x100, 3); SGB(0x8, 1); SGB(0x100, 3); SGB(0x8, 1); SGB(0x100, 3); SGB(0x8, 1); SGB(0x100, 3); SGB(0x8, 1); SGB(0x100, 4); SGB(0x8, 3);
            SB0();
            xpart();
            SB0();
#pragma unroll
            for (int j = 0; j < 3; ++j) { acc[0][0] = mfma16(w10[j][0], h10[j], acc[0][0]); acc[1][0] = mfma16(w10[j][1], h10[j], acc[1][0]); }
        }
        f32x4 mine[2];
#pragma unroll
        for (int j = 0; j < 4; ++j) {
            auto s_ = __builtin_amdgcn_permlane32_swap(__float_as_uint(acc[0][0][j]), __float_as_uint(acc[1][0][j]), false, false);
            auto t_ = __builtin_amdgcn_permlane16_swap(s_[0], s_[1], false, false);
            mine[0][j] = __uint_as_float(t_[0]); mine[1][j] = __uint_as_float(t_[1]);
        }
        u32x4 ov;
#pragma unroll
        for (int h = 0; h < 2; ++h) {
            float o[4];
#pragma unroll
            for (int j = 0; j < 4; ++j) o[j] = (tx[h][j] + mine[h][j]) + ((h ? e1 : e0)[j] + bf2f((u16)yq[h][j]));
            ov[2 * h] = pk2(o[0], o[1]); ov[2 * h + 1] = pk2(o[2], o[3]);
        }
        *(GAS u32x4*)(p.out + tok * DM + d8) = ov;
    }
}

__device__ __forceinline__ void service_waves(const Params& p, const int w, const int si, const char* code) {
    const int lane = threadIdx.x & 63;
    const int xcd = w & 7, idx = w >> 3, pn = 8 * (xcd & 1) + (idx & 7), cgp = 4 * (xcd >> 1) + (idx >> 3);
    const int wide = (cgp - 11) >> 31;
    const int f0 = (wide & (96 * cgp)) | (~wide & (352 + 64 * cgp)), nch = 8 + (wide & 4);
    const Lds lds; constexpr u32 lb = 0;
    u32 tq0, tq1, vw2;
    char* const ctl = p.ws + CTL_OFF;
    {
        const char* src[10]; u32 vo[10], str[10];
        u32 vw[3];
#pragma unroll
        for (int u = 0; u < 3; ++u) {
            const u32 e = 64 * u + lane, k = __umul24(e, 171u) >> 11;
            int c = (int)(e - __umul24(k, 12u)) ^ (int)(((k >> 3) & 1) << 1); if (c >= nch) c -= 8;
            vw[u] = __umul24(k, FF * 2) + 16 * c;
        }
#pragma unroll
        for (int i = 0; i < 10; ++i) {
            const int q = 4 * si + i;
            if (i < 4) { src[i] = (const char*)(p.x + (128 * pn + 8 * q) * DM); str[i] = 128; vo[i] = (lane >> 3) * 1024 + 16 * ((lane & 7) ^ (lane >> 3)); }
            else { src[i] = (const char*)((si >= 2 ? p.wu : p.wg) + f0 + 16 * (2 * (si & 1) + (i - 4) / 3) * FF); str[i] = 64 * FF * 2; vo[i] = vw[(i - 4) % 3]; }
        }
        auto issue = [&](u32 dst) {
#pragma unroll
            for (int i = 0; i < 10; ++i) { glds16<0>(src[i], vo[i], dst + (i < 4 ? 4 * si + i : 16 + 6 * si + (i - 4)) * 1024); src[i] += str[i]; }
        };
        const u32 d0 = lb;
        if (64 * si < CODE_LINES) touch<0>(code, (u32)min(64 * si + lane, CODE_LINES - 1) * 128, lb + L_DUM);
        issue(d0);
        auto warm = [&]() {
            const char* ta; u32 tstr;
            if (lane < 32) { ta = (const char*)(p.x + (128 * pn + 32 * (idx >> 3) + lane) * DM); tstr = 128; }
            else { const int e = lane - 32; ta = (const char*)(((e >> 4) ? p.wu : p.wg) + f0 + (size_t)(8 * (idx & 7) + ((e >> 1) & 7)) * FF) + ((e & 1) ? nch * 16 - 4 : 0); tstr = 64 * FF * 2; }
            { touch64(ta + (size_t)(2 + si) * tstr, lb + L_DUM); touch64(ta + (size_t)(si < 2 ? 6 + si : 2 + si) * tstr, lb + L_DUM); }
        };
        __builtin_amdgcn_s_sleep(8);
        issue(d0 + G1_STAGE);
        warm();
        BAR();
        BAR();
        tq0 = (u32)g2_tile(lane) * (16 * DM * 2); tq1 = (u32)g2_tile(64 + lane) * (16 * DM * 2);
        { const int k = lane >> 3, cp = lane & 7, f = (k >> 1) & 1; vw2 = k * (DM * 2) + 16 * ((((cp >> 1) ^ f) << 1) | (cp & 1)); }
        PIN(tq0); PIN(tq1); PIN(vw2);
        waitv<10 + TOUCH1>();
        BAR();
        issue(d0 + 2 * G1_STAGE);
        auto wd_touch = [&]() {
#pragma unroll
            for (int i = 0; i < 2; ++i) touch<0>(p.wd, (u32)((344 * (idx & 3) + min(64 * (2 * si + i) + lane, 343)) * (DM * 2) + (idx >> 2) * 128), lb + L_DUM);
        };
        constexpr int WT = 0, WL = 2;
        int slot = 0;
#pragma unroll
        for (int s = 0; s < 4; ++s) {
            if (s == 0) waitv<10 + TOUCH1>(); else if (s == 3) waitv<10 + WT>(); else waitv<10>();

            BAR();
            issue(d0 + slot * G1_STAGE);
            slot = slot == 2 ? 0 : slot + 1;
        }
        const u64 tag = *(LAS u64*)(lds + L_TAG);
        const char* const fap = ctl + (FA_W + 64 * (w >> 6)) * 8;
        waitv<10 + WT>();
        BAR();
        u64 fa = ~tag;
        const int pc = 4 * (si >> 1) + 1 + 2 * (si & 1);
        const char* g = p.ws + XT_OFF + ((size_t)(2 * w + (si >> 1)) * XS + (XP - 16) + (pc & 3) + 1) * 2;
        const u32 uo = L_U + pc * 4160;
        asm volatile("global_load_dwordx2 %0, %1, %2 sc1" : "+v"(fa) : "v"(lane * 8), "s"(fap) : "memory");
        issue(d0 + G1_STAGE);
        asm volatile("s_waitcnt vmcnt(10)" : "+v"(fa) :: "memory");
        BAR();
        if (!__all(fa == tag)) { WAITV0(); poll_flags(fap, 64, tag, lane); }
#pragma unroll
        for (int i = 0; i < 4; ++i) glds16<1>(g + i * 1024, lane * 16, uo + i * 1024);
        if (lane < 2) glds16<1>(g + 4096, lane * 16, uo + 4096);
        wd_touch();
        waitv<5 + WL>();
        BAR();
        waitv<WL>();
        if (lane < 15 - (pc & 3)) *(LAS u16*)(lds + uo + lane * 2) = 0;
        WAITL0();
        u32x4 a[5]; u32 b[5];
#pragma unroll
        for (int i = 0; i < 5; ++i) { const u32 o = uo + min(i * 64 + lane, 257) * 16; a[i] = *(LAS u32x4*)(lds + o); b[i] = *(LAS u32*)(lds + o - 4); }
        if (lane == 0) b[0] = 0;
#pragma unroll
        for (int i = 0; i < 5; ++i) {
            const u32x4 r = {__builtin_amdgcn_alignbit(a[i][0], b[i], 16), __builtin_amdgcn_alignbit(a[i][1], a[i][0], 16), __builtin_amdgcn_alignbit(a[i][2], a[i][1], 16), __builtin_amdgcn_alignbit(a[i][3], a[i][2], 16)};
            *(LAS u32x4*)(lds + uo - 4160 + min(i * 64 + lane, 257) * 16) = r;
        }
    }
    const u64 tag = *(LAS u64*)(lds + L_TAG);
    syn_arrive(SY_U, lane);
    if (w == 0 && si == 0) {
#pragma unroll 1
        for (u32 sp = 0; sp < SPIN_CAP; ++sp) {
            const char* f = ctl + (FA_W + lane) * 8;
            const u64 f0 = ld_u64(f), f1 = ld_u64(f + 512), f2 = ld_u64(f + 1024), f3 = ld_u64(f + 1536);
            if (__all(f0 == tag && f1 == tag && f2 == tag && f3 == tag)) break;
            __builtin_amdgcn_s_sleep(2);
        }
        if (lane == 0) st_u64(ctl, tag);
        WAITV0();
    }
    syn_await(SY_E0, 4); syn_await(SY_E1, 4);
    const u32 vl = lane * 16;
    const int rt = G2_RT(xcd, idx), ct = idx >> 2;
    const char* const srcw = (const char*)(p.wd + 64 * ct);
    const char* const srch = p.ws + H_OFF + (size_t)rt * 88 * 2048 + si * 4096;
    auto issue_w = [&](int s) {
        const u32 dst = lb + g2_slot(s) + 16384 + si * 4096;
#pragma unroll
        for (int i = 0; i < 2; ++i) {
            const char* sr = srcw + (u32)__builtin_amdgcn_readlane(s < 8 ? tq0 : tq1, (8 * s + 2 * si + i) & 63);
            glds16<0>(sr, vw2, dst + i * 2048); glds16<0>(sr + 8 * DM * 2, vw2 ^ 64u, dst + i * 2048 + 1024);
        }
    };
    auto issue_h = [&](int s, int slot) {
        const u32 dst = lb + slot + si * 4096;
#pragma unroll
        for (int i = 0; i < 4; ++i) glds16<1>(srch + s * 16384 + i * 1024, vl, dst + i * 1024);
    };
    syn_await(SY_F, 4);
    issue_w(0); issue_w(1); issue_w(2);
    if (si == 0) {
        poll_flags(ctl + (FH_W + rt * 64) * 8, 32, tag, lane);
        syn_arrive(SY_RDY, lane);
    } else syn_await(SY_RDY, 1);
    if (si == 1 && lane < 48) touch<1>(p.ws + H_OFF + (size_t)rt * 88 * 2048, (u32)((3 * 128 + 16 * ct + (lane >> 4) * 128 + (lane & 15)) * 128), lb + L_DUM);
    issue_h(0, g2_slot(0)); issue_h(1, g2_slot(1)); issue_h(2, g2_slot(2));
    waitv<8>();

    BAR();
    issue_h(3, g2_slot(3));
    issue_w(3);
    const char* const lfp = ctl + (FH_W + rt * 64 + 32 + (lane & 31)) * 8;
    const int ty = rt >> 2, tgy = (ty == 1 || ty == 2 || ty == 5 || ty == 6);
    const char* const fyp = ctl + (FY_W + (32 * ct + 8 * si + ((lane & 15) >> 1)) * 4 + (lane & 1) * 2 + tgy) * 8;
    u64 lf = ~tag, fy = ~tag;
    constexpr int TL = 1;
#pragma unroll
    for (int s = 0; s < 10; ++s) {
        constexpr int NW = 4;
        constexpr int SG = 4 + NW;
        constexpr int YW = 0;
        if (s == 0) waitv<12>(); else if (s == 1) waitv<9 + SG>(); else if (s == 6) waitv<1 + 2 * SG>(); else if (s == 3 || s == 4) waitv<2 * SG + TL>(); else if (s == 5) waitv<2 * SG>();
        else if (s == 2) asm volatile("s_waitcnt vmcnt(%1)" : "+v"(lf) : "n"(2 * SG) : "memory");
        else if (s == 7) asm volatile("s_waitcnt vmcnt(%1)" : "+v"(fy) : "n"(2 * SG) : "memory");
        else if (s == 8) waitv<2 + SG>(); else waitv<YW>();
        BAR();
        if (s == 0) asm volatile("global_load_dwordx2 %0, %1, off sc1" : "+v"(lf) : "v"(lfp) : "memory");
        if (s == 5) asm volatile("global_load_dwordx2 %0, %1, off sc1" : "+v"(fy) : "v"(fyp) : "memory");
        if (s == 2) {
            if (!__all(lf == tag)) poll_flags(ctl + (FH_W + rt * 64 + 32) * 8, 32, tag, lane);
            touch<1>(p.ws + H_OFF + (size_t)rt * 88 * 2048, (u32)((7 * 128 + 64 * ct + 16 * si + (lane & 15)) * 128), lb + g2_slot(6) + si * 4096);
        }
        if (s < 7) issue_h(s + 4, g2_slot(s + 4));
        if (s < 7) issue_w(s + 4);
        if (s == 7) {
            if (!__all(fy == tag)) poll_flags(fyp - lane * 8, 64, tag, lane);
#pragma unroll
            for (int i = 0; i < 2; ++i) glds16<1>(p.ws + YT_OFF + ((size_t)(64 * ct + 16 * si + 8 * i) * SL + 64 * rt) * 2, (lane >> 3) * (SL * 2) + (lane & 7) * 16, lb + L_Y + (16 * si + 8 * i) * 128);
        }
    }

}

__global__ __launch_bounds__(768) void k_fused(const u16* logdt, const u16* logA, const u16* aim, const u16* cre, const u16* cim, char* ws, const u16* x,
                                               const u16* wg, const u16* wu, const u16* wd, const u16* dskip, u16* out) {
    const int wid = __builtin_amdgcn_readfirstlane(threadIdx.x >> 6), w = blockIdx.x;
    Params p; p.x = x; p.logA = logA; p.aim = aim; p.cre = cre; p.cim = cim; p.logdt = logdt; p.dskip = dskip; p.wg = wg; p.wu = wu; p.wd = wd; p.out = out; p.ws = ws;
    u64 pc; asm volatile("s_getpc_b64 %0" : "=s"(pc));
    if (wid < 8) math_waves(p, w);
    else service_waves(p, w, wid - 8, (const char*)(pc & ~(u64)255));
}

extern "C" void kernel_launch(void* const* d_in, const int* in_sizes, int n_in, void* d_out, int out_size, void* d_ws, size_t ws_size, hipStream_t stream) {
    (void)hipFuncSetAttribute((const void*)k_fused, hipFuncAttributeMaxDynamicSharedMemorySize, LDS_BYTES);
    k_fused<<<256, 768, LDS_BYTES, stream>>>((const u16*)d_in[5], (const u16*)d_in[1], (const u16*)d_in[2], (const u16*)d_in[3], (const u16*)d_in[4], (char*)d_ws, (const u16*)d_in[0],
                                             (const u16*)d_in[8], (const u16*)d_in[9], (const u16*)d_in[10], (const u16*)d_in[6], (u16*)d_out);
}
```
